```python
import torch

def _softmax(x, axis=-1):
    return torch.softmax(x, dim=axis)

def _gelu(x):
    return torch.nn.functional.gelu(x, approximate='tanh')

def _silu(x):
    return torch.nn.functional.silu(x)

def _sigmoid(x):
    return torch.sigmoid(x)

def _layernorm(x, axis=-1, eps=1e-05):
    m = x.mean(dim=axis, keepdim=True)
    v = x.var(dim=axis, keepdim=True, unbiased=False)
    return (x - m) / torch.sqrt(v + eps)

def _scatter_add(tgt, idx, src, axis=0):
    return tgt.clone().index_add_(axis, idx, src)

def _segment_sum(src, seg, n):
    out = torch.zeros((n,) + src.shape[1:], dtype=src.dtype)
    return out.index_add_(0, seg, src)

def _pad(x, pw):
    flat = tuple((v for pair in reversed(pw) for v in pair))
    return torch.nn.functional.pad(x, flat)
kernel_name = 'recurrent_scan__spectral_fnet__mixer_gmlp__prompt'

def setup_inputs():

    _w = lambda a: (lambda t: t.to(torch.bfloat16) if t.is_floating_point() else t)(torch.as_tensor(a))

    def _A_setup():
        B, S, D = (1, 2048, 512)
        ns = torch.arange(S)[:, None] * torch.arange(S)[None, :]
        nd = torch.arange(D)[:, None] * torch.arange(D)[None, :]
        Ws_re = torch.cos(-2.0 * torch.pi * ns / S).to(float)
        Ws_im = torch.sin(-2.0 * torch.pi * ns / S).to(float)
        Wd_re = torch.cos(-2.0 * torch.pi * nd / D).to(float)
        Wd_im = torch.sin(-2.0 * torch.pi * nd / D).to(float)
        return {'x': _w((lambda *s: torch.randn(*s, dtype=torch.bfloat16) * 0.05)(B, S, D)), 'dft_seq_re': _w(Ws_re), 'dft_seq_im': _w(Ws_im), 'dft_hid_re': _w(Wd_re), 'dft_hid_im': _w(Wd_im)}
    _A_in = _A_setup()

    def _B_setup():
        B, S, D, F = (1, 2048, 512, 1376)
        Fh = F // 2
        return {'x': _w((lambda *s: torch.randn(*s, dtype=torch.bfloat16) * 0.05)(B, S, D)), 'w_in': _w((lambda *s: torch.randn(*s, dtype=torch.bfloat16) * 0.4)(D, 2 * Fh)), 'w_sp': _w(0.01 * (lambda *s: torch.randn(*s, dtype=torch.bfloat16) * 0.4)(S, S)), 'b_sp': _w(1.0 + 0.01 * (lambda *s: torch.randn(*s, dtype=torch.bfloat16) * 0.05)(S)), 'w_out': _w((lambda *s: torch.randn(*s, dtype=torch.bfloat16) * 0.05)(Fh, D))}
    _B_in = _B_setup()
    return {'A_x': _A_in['x'], 'A_dft_seq_re': _A_in['dft_seq_re'], 'A_dft_seq_im': _A_in['dft_seq_im'], 'A_dft_hid_re': _A_in['dft_hid_re'], 'A_dft_hid_im': _A_in['dft_hid_im'], 'B_x': _B_in['x'], 'B_w_in': _B_in['w_in'], 'B_w_sp': _B_in['w_sp'], 'B_b_sp': _B_in['b_sp'], 'B_w_out': _B_in['w_out']}

def reference(A_x, A_dft_seq_re, A_dft_seq_im, A_dft_hid_re, A_dft_hid_im, B_x, B_w_in, B_w_sp, B_b_sp, B_w_out):

    def _A_ref(x, dft_seq_re, dft_seq_im, dft_hid_re, dft_hid_im):
        a = torch.einsum('bsd,ed->bse', x, dft_hid_re)
        b = torch.einsum('bsd,ed->bse', x, dft_hid_im)
        y = torch.einsum('ts,bse->bte', dft_seq_re, a) - torch.einsum('ts,bse->bte', dft_seq_im, b)
        return y

    def _B_ref(x, w_in, w_sp, b_sp, w_out):
        B, S, D = x.shape
        F = w_in.shape[-1]
        Fh = F // 2
        z = _gelu(torch.einsum('bsd,df->bsf', x, w_in))
        z1 = z[..., :Fh].clone()
        z2 = _layernorm(z[..., Fh:], axis=-1)
        g = torch.einsum('ts,bsf->btf', w_sp, z2) + b_sp[None, :, None]
        return torch.einsum('bsf,fd->bsd', z1 * g, w_out)
    x = A_x
    xn = _layernorm(x, axis=-1)
    s = _A_ref(xn, A_dft_seq_re, A_dft_seq_im, A_dft_hid_re, A_dft_hid_im)
    g = _silu(_B_ref(xn, B_w_in, B_w_sp, B_b_sp, B_w_out))
    return x + s * g
```

```cpp
#include <hip/hip_runtime.h>
#include <hip/hip_bf16.h>
#include <stdint.h>
#include <type_traits>

typedef short bf16x8 __attribute__((ext_vector_type(8)));
typedef short bf16x4 __attribute__((ext_vector_type(4)));
typedef short v4i16_t __attribute__((ext_vector_type(4)));
typedef float f32x4 __attribute__((ext_vector_type(4)));
typedef float f32x2 __attribute__((ext_vector_type(2)));
typedef unsigned u32x4 __attribute__((ext_vector_type(4)));
typedef unsigned u32x2 __attribute__((ext_vector_type(2)));
typedef __bf16 bf16v2 __attribute__((ext_vector_type(2)));
typedef const __attribute__((address_space(1))) void* gptr_t;
typedef __attribute__((address_space(3))) void* lptr_t;
typedef __attribute__((address_space(3))) char* lchar_t;

#define WAIT_V(n) asm volatile("s_waitcnt vmcnt(%0)" ::"n"(n) : "memory")
#define WAIT_L0() asm volatile("s_waitcnt lgkmcnt(0)" ::: "memory")
#define BAR() __builtin_amdgcn_s_barrier()
#define SCHED() __builtin_amdgcn_sched_barrier(0)
#define MFMA(a, b, c) __builtin_amdgcn_mfma_f32_16x16x32_bf16(a, b, c, 0, 0, 0)

#define YSPLIT 2

constexpr int S_ = 2048, D_ = 512, F_ = 1376, FH = 688, NFT = 43;
constexpr int NT_ALL = 112;
constexpr size_t WS_BIMG = 0;
constexpr size_t WS_Z1 = (size_t)8 << 20;
constexpr size_t WS_H = (size_t)12 << 20;
constexpr size_t WS_Y = (size_t)16 << 20;
constexpr size_t WS_XST = (size_t)24 << 20;
constexpr size_t WS_ZST = WS_XST + 65536;
constexpr size_t WS_SALT = WS_ZST + 262144 + 65536;
constexpr size_t WS_DBG = (size_t)26 << 20;
constexpr int NPART = 2 * YSPLIT;

template <int I, int N, class F>
__device__ __forceinline__ void static_for(F&& f) {
  if constexpr (I < N) { f(std::integral_constant<int, I>{}); static_for<I + 1, N>(f); }
}
__device__ __forceinline__ unsigned pack2(float a, float b) {
  return __builtin_bit_cast(unsigned, __builtin_convertvector(f32x2{a, b}, bf16v2));
}
__device__ __forceinline__ float bflo(unsigned u) { return __uint_as_float(u << 16); }
__device__ __forceinline__ float bfhi(unsigned u) { return __uint_as_float(u & 0xffff0000u); }
__device__ __forceinline__ void glds16(const char* g, char* l) {
  __builtin_amdgcn_global_load_lds((gptr_t)g, (lptr_t)l, 16, 0, 0);
}
__device__ __forceinline__ void glds16o(const char* ubase, unsigned off, char* l) {
  asm volatile("" : "+v"(off));
  __builtin_amdgcn_global_load_lds((gptr_t)(ubase + off), (lptr_t)l, 16, 0, 0);
}
__device__ __forceinline__ void swap16(unsigned& a, unsigned& b) {
  auto r = __builtin_amdgcn_permlane16_swap(a, b, false, false);
  a = r[0]; b = r[1];
}
template <int CTRL>
__device__ __forceinline__ float dpp_add(float v) {
  return v + __int_as_float(__builtin_amdgcn_update_dpp(0, __float_as_int(v), CTRL, 0xf, 0xf, true));
}
__device__ __forceinline__ float row16_sum(float v) {
  v = dpp_add<0x128>(v); v = dpp_add<0x124>(v); v = dpp_add<0x122>(v); v = dpp_add<0x121>(v);
  return v;
}
__device__ __forceinline__ float wave_sum(float v) {
  v = row16_sum(v);
  const int i = __float_as_int(v);
  return __int_as_float(__builtin_amdgcn_readlane(i, 0)) + __int_as_float(__builtin_amdgcn_readlane(i, 16)) +
         __int_as_float(__builtin_amdgcn_readlane(i, 32)) + __int_as_float(__builtin_amdgcn_readlane(i, 48));
}
__device__ __forceinline__ float gelu_tanh(float v) {
  const float c0 = -2.3022081986f, c1 = -0.1029432397f;
  const float e = __builtin_amdgcn_exp2f(v * __builtin_fmaf(v * v, c1, c0));
  return v * __builtin_amdgcn_rcpf(1.0f + e);
}
__device__ __forceinline__ float silu(float v) {
  const float e = __builtin_amdgcn_exp2f(v * -1.4426950409f);
  return v * __builtin_amdgcn_rcpf(1.0f + e);
}
__device__ __forceinline__ bf16x8 lds_frag(const char* p) { return *(const bf16x8*)p; }
__device__ __forceinline__ bf16x4 lds_tr(const char* p) {
  return __builtin_bit_cast(bf16x4, __builtin_amdgcn_ds_read_tr16_b64_v4i16((__attribute__((address_space(3))) v4i16_t*)p));
}
__device__ __forceinline__ bf16x8 cat(bf16x4 a, bf16x4 b) { return __builtin_shufflevector(a, b, 0, 1, 2, 3, 4, 5, 6, 7); }

constexpr int AUX_SC1 = 16;
constexpr int RSRC_W3 = 0x00020000;
__device__ __forceinline__ void granule_store(__amdgpu_buffer_rsrc_t rs, int off, float v0, float v1, u32x2 tag) {
  u32x4 g = {__float_as_uint(v0), tag[0], __float_as_uint(v1), tag[1]};
  __builtin_amdgcn_raw_buffer_store_b128(g, rs, off, 0, AUX_SC1);
}
constexpr unsigned SPIN_MAX = 1u << 18;

#define TSTAMP(k, slot) do {} while (0)
#define TSTAMPW(k, slot, wv) do {} while (0)

__device__ __forceinline__ int part_start(int p) { return p < 3 ? 6 * p : 18 + 5 * (p - 3); }
__device__ __forceinline__ int part_cnt(int p) { return p < 3 ? 6 : 5; }

constexpr int A_STAGE = 36864, A_NS = 4, A_XO = 0, A_DO = 16384, A_WO = 24576;
constexpr int A_EX = A_NS * A_STAGE;
constexpr int A_CS = A_EX;
constexpr int A_XRS = A_CS + 2560;
constexpr int A_XNM = A_XRS + 512;
constexpr int A_ZPS = A_XNM + 512;
constexpr int A_ZPQ = A_ZPS + 1024;
constexpr int A_ZRS = A_ZPQ + 1024;
constexpr int A_ZNM = A_ZRS + 512;
constexpr int A_LDS = A_ZNM + 512;

template <bool Z2>
__device__ __forceinline__ void phaseA(const char* __restrict__ x, const char* __restrict__ wd, const char* __restrict__ win,
                                       char* __restrict__ ws, char* shm, int panel, int j) {
  const int tid = threadIdx.x, w = __builtin_amdgcn_readfirstlane(tid >> 6), lane = tid & 63;
  const int wr = w >> 1, wc = w & 1, c = lane & 15, q = lane >> 4;
  const int part = j & 7, fstart = part_start(part), fcnt = part_cnt(part);

  u32x2 salt; u32x4 xr;
  {
    const char* ps = ws + WS_SALT + panel * 128;
    const char* pr = x + (size_t)(panel * 128 + 8 * j + w) * 1024 + lane * 16;
    asm volatile("global_load_dwordx2 %0, %1, off sc1" : "=&v"(salt) : "v"(ps) : "memory");
    asm volatile("global_load_dwordx4 %0, %1, off" : "=&v"(xr) : "v"(pr) : "memory");
  }
  unsigned ox[2], od, ow[2];
#pragma unroll
  for (int i = 0; i < 2; ++i) {
    const int row = 8 * (w + 8 * i) + (lane >> 3);
    ox[i] = (unsigned)(panel * 128 + row) * 1024u + (unsigned)(((lane & 7) ^ ((row >> 1) & 7)) << 4);
  }
  {
    const int row = 8 * w + (lane >> 3);
    od = (unsigned)((j & 7) * 64 + row) * 1024u + (unsigned)(((lane & 7) ^ ((row >> 1) & 7)) << 4);
  }
#pragma unroll
  for (int i = 0; i < 2; ++i) {
    const int P = (i == 0) ? w : 8 + (w & 3);
    const int o = P * 1024 + lane * 16, row = o / 192, ch = (o % 192) >> 4;
    const int u = (ch >> 1) ^ ((row >> 3) & 1);
    const int ft = fstart + (u < fcnt ? u : fcnt - 1);
    ow[i] = (unsigned)row * 2752u + (unsigned)((Z2 ? FH * 2 : 0) + ft * 32 + (ch & 1) * 16);
  }
  auto issue = [&](int t) {
    char* st = shm + (t % A_NS) * A_STAGE;
    const char* xb = x + t * 128;
    const char* db = wd + t * 128;
    const char* wb = win + (size_t)t * (64 * 2752);
    asm volatile("" : "+s"(xb), "+s"(db), "+s"(wb));
    glds16o(xb, ox[0], st + A_XO + w * 1024);
    glds16o(xb, ox[1], st + A_XO + (w + 8) * 1024);
    glds16o(db, od, st + A_DO + w * 1024);
    glds16o(wb, ow[0], st + A_WO + w * 1024);
    if (w < 4) glds16o(wb, ow[1], st + A_WO + (8 + w) * 1024);
  };
#pragma unroll
  for (int t = 0; t < A_NS; ++t) issue(t);

  asm volatile("s_waitcnt vmcnt(16)" : "+v"(salt), "+v"(xr)::"memory");
  const unsigned long long tag64 = (((unsigned long long)salt[1] << 32) | salt[0]) + 0x9E3779B97F4A7C15ull;
  const u32x2 tag = {(unsigned)tag64, (unsigned)(tag64 >> 32)};
  const __amdgpu_buffer_rsrc_t rsx = __builtin_amdgcn_make_buffer_rsrc((void*)(ws + WS_XST), 0, 65536 + 262144, RSRC_W3);
  {
    float v[8];
#pragma unroll
    for (int i = 0; i < 4; ++i) { v[2 * i] = bflo(xr[i]); v[2 * i + 1] = bfhi(xr[i]); }
    float s = ((v[0] + v[1]) + (v[2] + v[3])) + ((v[4] + v[5]) + (v[6] + v[7]));
    const float mean = wave_sum(s) * (1.0f / D_);
    float ss = 0.f;
#pragma unroll
    for (int i = 0; i < 8; ++i) { const float d = v[i] - mean; ss = __builtin_fmaf(d, d, ss); }
    const float var = wave_sum(ss) * (1.0f / D_);
    const float rs = __builtin_amdgcn_rsqf(var + 1e-5f);
    if (lane == 0) granule_store(rsx, (panel * 128 + 8 * j + w) * 16, rs, -mean * rs, tag);
  }

  f32x4 acc[2][5] = {};
  f32x4 cs[5] = {};
  bf16x8 fx[2][2], fd[2][2], fw[2][3];
  const bf16x8 ones = {0x3F80, 0x3F80, 0x3F80, 0x3F80, 0x3F80, 0x3F80, 0x3F80, 0x3F80};
  int ax[2], ad[2], aw[3];
#pragma unroll
  for (int ks = 0; ks < 2; ++ks) {
    const int sw = ((ks * 4 + q) ^ ((c >> 1) & 7)) << 4;
    ax[ks] = A_XO + (32 * wr + c) * 128 + sw;
    ad[ks] = A_DO + (32 * wc + c) * 128 + sw;
  }
#pragma unroll
  for (int i = 0; i < 3; ++i) aw[i] = A_WO + (8 * q + (c >> 2)) * 192 + (((3 * wc + i) ^ (q & 1)) << 5) + (c & 3) * 8;
  auto rd = [&](int set, int t, int ks) {
    const char* st = shm + (t % A_NS) * A_STAGE;
#pragma unroll
    for (int m = 0; m < 2; ++m) fx[set][m] = lds_frag(st + ax[ks] + m * 2048);
#pragma unroll
    for (int i = 0; i < 2; ++i) fd[set][i] = lds_frag(st + ad[ks] + i * 2048);
#pragma unroll
    for (int i = 0; i < 3; ++i) fw[set][i] = cat(lds_tr(st + aw[i] + ks * 6144), lds_tr(st + aw[i] + ks * 6144 + 768));
  };
  auto mm = [&](int set, int kstep) {
#pragma unroll
    for (int m = 0; m < 2; ++m) {
#pragma unroll
      for (int i = 0; i < 2; ++i) acc[m][i] = MFMA(fx[set][m], fd[set][i], acc[m][i]);
#pragma unroll
      for (int i = 0; i < 3; ++i)
        acc[m][2 + i] = Z2 ? MFMA(fx[set][m], fw[set][i], acc[m][2 + i]) : MFMA(fw[set][i], fx[set][m], acc[m][2 + i]);
    }
    if ((kstep & 3) == wr) {
#pragma unroll
      for (int i = 0; i < 2; ++i) cs[i] = MFMA(ones, fd[set][i], cs[i]);
#pragma unroll
      for (int i = 0; i < 3; ++i) cs[2 + i] = Z2 ? MFMA(ones, fw[set][i], cs[2 + i]) : MFMA(fw[set][i], ones, cs[2 + i]);
    }
  };
  WAIT_V(13);
  BAR();
  rd(0, 0, 0);
  static_for<0, 8>([&](auto T) {
    constexpr int t = T.value;
    rd(1, t, 1);
    mm(0, 2 * t);
    WAIT_L0();
    if constexpr (t < 7) {
      constexpr int rem = (t + 3 < 7 ? t + 3 : 7) - (t + 1);
      WAIT_V(rem * 4);
    }
    BAR();
    if constexpr (t + A_NS < 8) issue(t + A_NS);
    if constexpr (t < 7) rd(0, t + 1, 0);
    mm(1, 2 * t + 1);
  });

  float* csum = (float*)(shm + A_CS);
  float* xrs = (float*)(shm + A_XRS);
  float* xnm = (float*)(shm + A_XNM);
#pragma unroll
  for (int i = 0; i < 5; ++i) {
    if (i < 2 || Z2) { if (q == 0) csum[((wc * 5 + i) * 16 + c) * 4 + wr] = cs[i][0]; }
    else if (c == 0) {
#pragma unroll
      for (int r = 0; r < 4; ++r) csum[((wc * 5 + i) * 16 + 4 * q + r) * 4 + wr] = cs[i][r];
    }
  }
  if (w < 2) {
    for (unsigned spin = 0; spin < SPIN_MAX; ++spin) {
      const u32x4 g = __builtin_amdgcn_raw_buffer_load_b128(rsx, (panel * 128 + tid) * 16, 0, AUX_SC1);
      const bool ok = (g[1] == tag[0]) & (g[3] == tag[1]);
      xrs[tid] = __uint_as_float(g[0]); xnm[tid] = __uint_as_float(g[2]);
      if (__all(ok)) break;
      __builtin_amdgcn_s_sleep(1);
    }
  }
  __syncthreads();

  char* bimg = ws + WS_BIMG + (size_t)(panel * 4 + wr) * (NT_ALL * 1024) + (c + 16 * (((q & 1) << 1) | (q >> 1))) * 16;
  f32x4 rs4[2], nm4[2];
#pragma unroll
  for (int m = 0; m < 2; ++m) {
    rs4[m] = *(const f32x4*)(xrs + 32 * wr + 16 * m + 4 * q);
    nm4[m] = *(const f32x4*)(xnm + 32 * wr + 16 * m + 4 * q);
  }
  auto colsum1 = [&](int i) { const f32x4 t = *(const f32x4*)(csum + ((wc * 5 + i) * 16 + c) * 4); return (t[0] + t[1]) + (t[2] + t[3]); };
  auto store_bt = [&](const f32x4& v0, const f32x4& v1, int ntile) {
    unsigned a0 = pack2(v0[0], v0[1]), a1 = pack2(v0[2], v0[3]), b0 = pack2(v1[0], v1[1]), b1 = pack2(v1[2], v1[3]);
    swap16(a0, b0); swap16(a1, b1);
    *(u32x4*)(bimg + ntile * 1024) = u32x4{a0, a1, b0, b1};
  };
  auto epi_ab = [&]() {
#pragma unroll
    for (int i = 0; i < 2; ++i) {
      const float cv = colsum1(i);
      store_bt(rs4[0] * acc[0][i] + nm4[0] * cv, rs4[1] * acc[1][i] + nm4[1] * cv, (j >> 3) * 32 + (j & 7) * 4 + 2 * wc + i);
    }
  };

  if constexpr (!Z2) {
    char* z1 = ws + WS_Z1 + (size_t)(panel * 4 + wr) * (NFT * 1024) + lane * 16;
    float rl[2], nl[2];
#pragma unroll
    for (int m = 0; m < 2; ++m) { rl[m] = xrs[32 * wr + 16 * m + c]; nl[m] = xnm[32 * wr + 16 * m + c]; }
#pragma unroll
    for (int i = 0; i < 3; ++i) {
      if (3 * wc + i < fcnt) {
        f32x4 cv;
#pragma unroll
        for (int r = 0; r < 4; ++r) { const f32x4 t = *(const f32x4*)(csum + ((wc * 5 + 2 + i) * 16 + 4 * q + r) * 4); cv[r] = (t[0] + t[1]) + (t[2] + t[3]); }
        unsigned p[4];
#pragma unroll
        for (int m = 0; m < 2; ++m) {
          const f32x4 v = rl[m] * acc[m][2 + i] + nl[m] * cv;
          p[2 * m] = pack2(gelu_tanh(v[0]), gelu_tanh(v[1]));
          p[2 * m + 1] = pack2(gelu_tanh(v[2]), gelu_tanh(v[3]));
        }
        *(u32x4*)(z1 + (fstart + 3 * wc + i) * 1024) = u32x4{p[0], p[1], p[2], p[3]};
      }
    }
    epi_ab();
  } else {
    f32x4 zv[2][3];
    f32x4 ssum[2] = {}, ssq[2] = {};
#pragma unroll
    for (int i = 0; i < 3; ++i) {
      const float cv = colsum1(2 + i);
      const float valid = (3 * wc + i < fcnt) ? 1.0f : 0.0f;
#pragma unroll
      for (int m = 0; m < 2; ++m) {
        const f32x4 v = rs4[m] * acc[m][2 + i] + nm4[m] * cv;
#pragma unroll
        for (int r = 0; r < 4; ++r) zv[m][i][r] = gelu_tanh(v[r]) * valid;
        ssum[m] += zv[m][i];
        ssq[m] += zv[m][i] * zv[m][i];
      }
    }
    float* zps = (float*)(shm + A_ZPS);
    float* zpq = (float*)(shm + A_ZPQ);
    float* zrs = (float*)(shm + A_ZRS);
    float* znm = (float*)(shm + A_ZNM);
#pragma unroll
    for (int m = 0; m < 2; ++m) {
#pragma unroll
      for (int r = 0; r < 4; ++r) { ssum[m][r] = row16_sum(ssum[m][r]); ssq[m][r] = row16_sum(ssq[m][r]); }
      if (c == 0) {
        *(f32x4*)(zps + wc * 128 + 32 * wr + 16 * m + 4 * q) = ssum[m];
        *(f32x4*)(zpq + wc * 128 + 32 * wr + 16 * m + 4 * q) = ssq[m];
      }
    }
    __syncthreads();
    const int zoff = 65536 + (panel * 8 * 128 + tid) * 16;
    if (w < 2) granule_store(rsx, zoff + part * 2048, zps[tid] + zps[128 + tid], zpq[tid] + zpq[128 + tid], tag);
    epi_ab();
    if (w < 2) {
      for (unsigned spin = 0; spin < SPIN_MAX; ++spin) {
        bool ok = true; float s = 0.f, sq = 0.f;
#pragma unroll
        for (int p = 0; p < 8; ++p) {
          const u32x4 g = __builtin_amdgcn_raw_buffer_load_b128(rsx, zoff + p * 2048, 0, AUX_SC1);
          ok = ok & (g[1] == tag[0]) & (g[3] == tag[1]);
          s += __uint_as_float(g[0]); sq += __uint_as_float(g[2]);
        }
        const float mean = s * (1.0f / FH);
        const float var = __builtin_fmaxf(sq * (1.0f / FH) - mean * mean, 0.0f);
        const float rs = __builtin_amdgcn_rsqf(var + 1e-5f);
        zrs[tid] = rs; znm[tid] = -mean * rs;
        if (__all(ok)) break;
        __builtin_amdgcn_s_sleep(1);
      }
    }
    __syncthreads();
    if (j == 0 && tid == 0)
      __hip_atomic_store((unsigned long long*)(ws + WS_SALT + panel * 128), tag64, __ATOMIC_RELAXED, __HIP_MEMORY_SCOPE_AGENT);
    f32x4 zr[2], zn[2];
#pragma unroll
    for (int m = 0; m < 2; ++m) {
      zr[m] = *(const f32x4*)(zrs + 32 * wr + 16 * m + 4 * q);
      zn[m] = *(const f32x4*)(znm + 32 * wr + 16 * m + 4 * q);
    }
#pragma unroll
    for (int i = 0; i < 3; ++i)
      if (3 * wc + i < fcnt) store_bt(zv[0][i] * zr[0] + zn[0], zv[1][i] * zr[1] + zn[1], 64 + fstart + 3 * wc + i);
  }
}

__global__ __launch_bounds__(512, 2) void kA(const char* __restrict__ x, const char* __restrict__ wdre, const char* __restrict__ wdim,
                                             const char* __restrict__ win, char* __restrict__ ws) {
  extern __shared__ __attribute__((aligned(1024))) char shm[];
  TSTAMP(0, 0);
  const int xcd = blockIdx.x & 7, i = blockIdx.x >> 3;
  const int panel = (xcd & 1) * 8 + (i >> 2), j = (xcd >> 1) * 4 + (i & 3);
  if (j < 8) phaseA<true>(x, wdre, win, ws, shm, panel, j);
  else phaseA<false>(x, wdim, win, ws, shm, panel, j);
  TSTAMP(0, 1);
}

template <int MT, int NT, int NS, int NSTG, bool DELAY>
struct GemmB {
  static constexpr int ABYTES = 32 * MT * 128, BBYTES = 4 * NT * 1024, STAGE = ABYTES + BBYTES, NP = MT + NT;

  static __device__ __forceinline__ void producer(char* shm, const char* A, const char* B, int nt0, int ncnt, int pw, int lane) {
    unsigned oa[MT], ob[NT];
#pragma unroll
    for (int i = 0; i < MT; ++i) {
      const int row = (pw * MT + i) * 8 + (lane >> 3);
      oa[i] = (unsigned)row * 4096u + (unsigned)(((lane & 7) ^ ((row >> 1) & 7)) << 4);
    }
#pragma unroll
    for (int i = 0; i < NT; ++i) {
      const int n = (pw & 1) * NT + i;
      ob[i] = (unsigned)(((pw >> 1) * NT_ALL + nt0 + (n < ncnt ? n : ncnt - 1)) * 1024 + lane * 16);
    }
    auto issue = [&](int t, int slot) {
      char* st = shm + slot * STAGE;
      const char* ab = A + t * 128;
      const char* bb = B + (size_t)t * (2 * NT_ALL * 1024);
      asm volatile("" : "+s"(ab), "+s"(bb));
#pragma unroll
      for (int i = 0; i < MT; ++i) glds16o(ab, oa[i], st + (pw * MT + i) * 1024);
#pragma unroll
      for (int i = 0; i < NT; ++i) glds16o(bb, ob[i], st + ABYTES + (pw * NT + i) * 1024);
    };
    constexpr int PRE = DELAY ? NS - 1 : NS;
#pragma unroll
    for (int t = 0; t < PRE; ++t) issue(t, t);
    WAIT_V((PRE - 1) * NP); BAR();
    int slot = PRE % NS;
    for (int t = 0; t < NSTG - PRE; ++t) {
      WAIT_V((PRE - 2) * NP); BAR();
      issue(t + PRE, slot);
      slot = (slot + 1 == NS) ? 0 : slot + 1;
    }
    static_for<0, PRE>([&](auto I) {
      constexpr int rem = PRE - 2 - I.value;
      WAIT_V((rem > 0 ? rem : 0) * NP); BAR();
    });
  }

  static __device__ __forceinline__ void consumer(char* shm, f32x4 (&acc)[MT][NT], int wr, int wc, int lane) {
    const int c = lane & 15, q = lane >> 4;
    bf16x8 fa[2][MT], fb[2][NT];
    int aoff[2];
#pragma unroll
    for (int ks = 0; ks < 2; ++ks) aoff[ks] = (wr * 16 * MT + c) * 128 + (((ks * 4 + q) ^ ((c >> 1) & 7)) << 4);
    const int boff = ABYTES + wc * NT * 1024 + lane * 16;
    auto rd = [&](int set, int so, int ks) {
      const char* st = shm + so;
#pragma unroll
      for (int i = 0; i < (MT > NT ? MT : NT); ++i) {
        if (i < NT) fb[set][i] = lds_frag(st + boff + ks * (2 * NT * 1024) + i * 1024);
        if (i < MT) fa[set][i] = lds_frag(st + aoff[ks] + i * 2048);
      }
    };
    auto mm = [&](int set) {
#pragma unroll
      for (int m = 0; m < MT; ++m)
#pragma unroll
        for (int n = 0; n < NT; ++n) acc[m][n] = MFMA(fb[set][n], fa[set][m], acc[m][n]);
    };
    auto interleave = [&]() {
#pragma unroll
      for (int i = 0; i < MT + NT; ++i) { __builtin_amdgcn_sched_group_barrier(0x8, 1, 0); __builtin_amdgcn_sched_group_barrier(0x100, 1, 0); }
      __builtin_amdgcn_sched_group_barrier(0x8, MT * NT - MT - NT, 0);
    };
    BAR();
    rd(0, 0, 0);
    __builtin_amdgcn_s_waitcnt(0xc07f); SCHED();
    int so = 0;
    for (int t = 0; t < NSTG; ++t) {
      rd(1, so, 1); mm(0); interleave(); SCHED();
      if (!DELAY) WAIT_L0();
      BAR(); SCHED();
      so = (so + STAGE == NS * STAGE) ? 0 : so + STAGE;
      rd(0, so, 0);
      mm(1); interleave(); SCHED();
    }
  }
};

typedef GemmB<8, 4, 3, 16, false> GemmY;
typedef GemmB<4, 3, 5, 32, true> GemmG;
constexpr int B_LDS = (GemmY::STAGE * (YSPLIT == 1 ? 4 : 3) > GemmG::STAGE * 5) ? GemmY::STAGE * (YSPLIT == 1 ? 4 : 3) : GemmG::STAGE * 5;

__global__ __launch_bounds__(512, 2) void kB(const char* __restrict__ wsre, const char* __restrict__ wsim, const char* __restrict__ wsp,
                                             const char* __restrict__ bsp, char* __restrict__ ws) {
  extern __shared__ __attribute__((aligned(1024))) char shm[];
  TSTAMP(1, 0);
  const int tid = threadIdx.x, w = __builtin_amdgcn_readfirstlane(tid >> 6), lane = tid & 63;
  const int xcd = blockIdx.x & 7, j = blockIdx.x >> 3;
  const int c = lane & 15, q = lane >> 4;
  if (xcd < 4) {
    constexpr int MT = YSPLIT == 1 ? 4 : 8;
    const int mat = xcd >> 1, eg = j & 3;
    const int row0 = YSPLIT == 1 ? ((xcd & 1) * 8 + (j >> 2)) * 128 : (j >> 2) * 256;
    const int kh = YSPLIT == 1 ? 0 : (xcd & 1);
    if (w >= 4) {
      GemmY::producer(shm, (mat ? wsim : wsre) + (size_t)row0 * 4096 + kh * 2048, ws + WS_BIMG + (size_t)kh * 32 * (NT_ALL * 1024),
                      mat * 32 + eg * 8, 8, w - 4, lane);
    } else {
      const int wr = w >> 1, wc = w & 1;
      f32x4 acc[MT][4] = {};
      GemmY::consumer(shm, acc, wr, wc, lane);
      char* yp = ws + WS_Y + (size_t)(YSPLIT == 1 ? mat : mat * 2 + kh) * (S_ * D_ * 2) +
                 (size_t)(row0 + wr * 16 * MT + c) * 1024 + (eg * 128 + wc * 64 + 16 * (q & 1) + 8 * (q >> 1)) * 2;
#pragma unroll
      for (int m = 0; m < MT; ++m)
#pragma unroll
        for (int h = 0; h < 2; ++h) {
          const f32x4 v0 = acc[m][2 * h], v1 = acc[m][2 * h + 1];
          unsigned a0 = pack2(v0[0], v0[1]), a1 = pack2(v0[2], v0[3]), b0 = pack2(v1[0], v1[1]), b1 = pack2(v1[2], v1[3]);
          swap16(a0, b0); swap16(a1, b1);
          *(u32x4*)(yp + m * 16384 + h * 64) = u32x4{a0, a1, b0, b1};
        }
    }
  } else {
    const int panel = (xcd - 4) * 4 + (j >> 3), part = j & 7;
    const int fstart = part_start(part), fcnt = part_cnt(part);
    if (w >= 4) {
      GemmG::producer(shm, wsp + (size_t)panel * 128 * 4096, ws + WS_BIMG, 64 + fstart, fcnt, w - 4, lane);
    } else {
      const int wr = w >> 1, wc = w & 1;
      u32x4 z1[2][3]; float bs[4];
#pragma unroll
      for (int pp = 0; pp < 2; ++pp)
#pragma unroll
        for (int n = 0; n < 3; ++n) {
          const int u = 3 * wc + n, ft = fstart + (u < fcnt ? u : fcnt - 1);
          z1[pp][n] = *(const u32x4*)(ws + WS_Z1 + (size_t)((panel * 4 + wr * 2 + pp) * NFT + ft) * 1024 + lane * 16);
        }
#pragma unroll
      for (int m = 0; m < 4; ++m) bs[m] = bflo(*(const unsigned short*)(bsp + (panel * 128 + wr * 64 + m * 16 + c) * 2));
      f32x4 acc[4][3] = {};
      GemmG::consumer(shm, acc, wr, wc, lane);
#pragma unroll
      for (int n = 0; n < 3; ++n) {
        const int u = 3 * wc + n;
        if (u < fcnt) {
          const int ft = fstart + u;
#pragma unroll
          for (int m = 0; m < 4; ++m) {
            const unsigned zl = z1[m >> 1][n][2 * (m & 1)], zh = z1[m >> 1][n][2 * (m & 1) + 1];
            const f32x4 g = acc[m][n] + bs[m];
            const u32x2 o = {pack2(bflo(zl) * g[0], bfhi(zl) * g[1]), pack2(bflo(zh) * g[2], bfhi(zh) * g[3])};
            *(u32x2*)(ws + WS_H + (size_t)((panel * 8 + wr * 4 + m) * 22 + (ft >> 1)) * 1024 + lane * 16 + (ft & 1) * 8) = o;
          }
        }
      }
    }
  }
  TSTAMP(1, 1);
}

constexpr int C_LDS = 88 * 1024;
__global__ __launch_bounds__(512, 2) void kC(const char* __restrict__ x, const char* __restrict__ wout, const char* __restrict__ ws_c,
                                             char* __restrict__ out
) {
  extern __shared__ __attribute__((aligned(1024))) char shm[];
  TSTAMP(2, 0);
  const int tid = threadIdx.x, w = __builtin_amdgcn_readfirstlane(tid >> 6), lane = tid & 63;
  const int xcd = blockIdx.x & 7, i = blockIdx.x >> 3;
  const int tb = xcd * 4 + (i >> 3), G = i & 7;
  if (w >= 4) {
    const int pw = w - 4;
    const int rr = lane >> 3;
    const unsigned lo = (unsigned)rr * 1024u + (unsigned)(G * 128 + (((lane & 7) ^ (2 * ((rr >> 1) & 3))) << 4));
#pragma unroll
    for (int ch = 0; ch < 3; ++ch)
#pragma unroll
      for (int k = 0; k < 6; ++k) { const int P = 24 * ch + 6 * pw + k; glds16o(wout + (size_t)P * 8192, lo, shm + P * 1024); }
#pragma unroll
    for (int k = 0; k < 4; ++k) { int P = 72 + 4 * pw + k; P = P < 85 ? P : 85; glds16o(wout + (size_t)P * 8192, lo, shm + P * 1024); }
    WAIT_V(16); BAR();
    WAIT_V(10); BAR();
    WAIT_V(4); BAR();
    WAIT_V(0); BAR();
  } else {
    const int c = lane & 15, q = lane >> 4;
    const char* hp = ws_c + WS_H + (size_t)(tb * 4 + w) * (22 * 1024) + lane * 16;
    bf16x8 hf[22];
#pragma unroll
    for (int kb = 0; kb < 21; ++kb) hf[kb] = *(const bf16x8*)(hp + kb * 1024);
    hf[21] = cat(*(const bf16x4*)(hp + 21 * 1024), bf16x4{0, 0, 0, 0});
    const size_t eo = (size_t)(tb * 64 + w * 16 + c) * 1024 + (G * 64 + 16 * (q & 1) + 8 * (q >> 1)) * 2;
    u32x4 xv[2], yv[NPART][2];
#pragma unroll
    for (int h = 0; h < 2; ++h) {
      xv[h] = *(const u32x4*)(x + eo + h * 64);
#pragma unroll
      for (int p = 0; p < NPART; ++p) yv[p][h] = *(const u32x4*)(ws_c + WS_Y + (size_t)p * (S_ * D_ * 2) + eo + h * 64);
    }
    int at[4];
    {
      const int r = c >> 2, sg = (2 * (q & 1) + (r >> 1)) & 3;
#pragma unroll
      for (int T = 0; T < 4; ++T) at[T] = (4 * q + r) * 128 + ((T ^ sg) << 5) + (c & 3) * 8;
    }
    f32x4 acc[4] = {};
    bf16x8 wf[3][4];
    auto rdw = [&](auto KB) {
      constexpr int kb = KB.value;
      if constexpr (kb == 0 || kb == 6 || kb == 12 || kb == 18) { BAR(); }
#pragma unroll
      for (int T = 0; T < 4; ++T) {
        const bf16x4 lo = lds_tr(shm + at[T] + kb * 4096);
        const bf16x4 hi = (kb < 21) ? lds_tr(shm + at[T] + kb * 4096 + 2048) : bf16x4{0, 0, 0, 0};
        wf[kb % 3][T] = cat(lo, hi);
      }
    };
    rdw(std::integral_constant<int, 0>{});
    rdw(std::integral_constant<int, 1>{});
    SCHED();
    static_for<0, 22>([&](auto KB) {
      constexpr int kb = KB.value;
      if constexpr (kb + 2 < 22) rdw(std::integral_constant<int, kb + 2>{});
#pragma unroll
      for (int T = 0; T < 4; ++T) acc[T] = MFMA(wf[kb % 3][T], hf[kb], acc[T]);
#pragma unroll
      for (int T = 0; T < 4; ++T) { __builtin_amdgcn_sched_group_barrier(0x8, 1, 0); __builtin_amdgcn_sched_group_barrier(0x100, 2, 0); }
      SCHED();
    });
#pragma unroll
    for (int h = 0; h < 2; ++h) {
      float o[8];
#pragma unroll
      for (int r = 0; r < 4; ++r) {
        unsigned a = __float_as_uint(acc[2 * h][r]), b = __float_as_uint(acc[2 * h + 1][r]);
        swap16(a, b);
        o[r] = __uint_as_float(a); o[4 + r] = __uint_as_float(b);
      }
      unsigned res[4];
#pragma unroll
      for (int d = 0; d < 4; ++d) {
        float yl, yh;
        if constexpr (NPART == 2) {
          yl = bflo(yv[0][h][d]) - bflo(yv[1][h][d]); yh = bfhi(yv[0][h][d]) - bfhi(yv[1][h][d]);
        } else {
          yl = (bflo(yv[0][h][d]) + bflo(yv[1][h][d])) - (bflo(yv[2][h][d]) + bflo(yv[3][h][d]));
          yh = (bfhi(yv[0][h][d]) + bfhi(yv[1][h][d])) - (bfhi(yv[2][h][d]) + bfhi(yv[3][h][d]));
        }
        res[d] = pack2(bflo(xv[h][d]) + yl * silu(o[2 * d]), bfhi(xv[h][d]) + yh * silu(o[2 * d + 1]));
      }
      *(u32x4*)(out + eo + h * 64) = u32x4{res[0], res[1], res[2], res[3]};
    }
  }
  TSTAMP(2, 1);
}

extern "C" void kernel_launch(void* const* d_in, const int* in_sizes, int n_in, void* d_out, int out_size, void* d_ws, size_t ws_size,
                              hipStream_t stream) {
  const char* x = (const char*)d_in[0];
  const char* wsre = (const char*)d_in[1];
  const char* wsim = (const char*)d_in[2];
  const char* wdre = (const char*)d_in[3];
  const char* wdim = (const char*)d_in[4];
  const char* win = (const char*)d_in[6];
  const char* wsp = (const char*)d_in[7];
  const char* bsp = (const char*)d_in[8];
  const char* wout = (const char*)d_in[9];
  char* ws = (char*)d_ws;
  (void)hipFuncSetAttribute((const void*)kA, hipFuncAttributeMaxDynamicSharedMemorySize, A_LDS);
  (void)hipFuncSetAttribute((const void*)kB, hipFuncAttributeMaxDynamicSharedMemorySize, B_LDS);
  (void)hipFuncSetAttribute((const void*)kC, hipFuncAttributeMaxDynamicSharedMemorySize, C_LDS);
  kA<<<256, 512, A_LDS, stream>>>(x, wdre, wdim, win, ws);
  kB<<<256, 512, B_LDS, stream>>>(wsre, wsim, wsp, bsp, ws);
  kC<<<256, 512, C_LDS, stream>>>(x, wout, ws, (char*)d_out);
}
```

```cpp
#define AUXY 2
#define AUXG 2
#include <hip/hip_runtime.h>
#include <hip/hip_bf16.h>
#include <stdint.h>
#include <type_traits>

typedef short bf16x8 __attribute__((ext_vector_type(8)));
typedef short bf16x4 __attribute__((ext_vector_type(4)));
typedef short v4i16_t __attribute__((ext_vector_type(4)));
typedef float f32x4 __attribute__((ext_vector_type(4)));
typedef float f32x2 __attribute__((ext_vector_type(2)));
typedef unsigned u32x4 __attribute__((ext_vector_type(4)));
typedef unsigned u32x2 __attribute__((ext_vector_type(2)));
typedef __bf16 bf16v2 __attribute__((ext_vector_type(2)));
typedef const __attribute__((address_space(1))) void* gptr_t;
typedef __attribute__((address_space(3))) void* lptr_t;
typedef __attribute__((address_space(3))) char* lchar_t;

#define WAIT_V(n) asm volatile("s_waitcnt vmcnt(%0)" ::"n"(n) : "memory")
#define WAIT_L0() asm volatile("s_waitcnt lgkmcnt(0)" ::: "memory")
#define BAR() __builtin_amdgcn_s_barrier()
#define SCHED() __builtin_amdgcn_sched_barrier(0)
#define MFMA(a, b, c) __builtin_amdgcn_mfma_f32_16x16x32_bf16(a, b, c, 0, 0, 0)

#define YSPLIT 2

constexpr int S_ = 2048, D_ = 512, F_ = 1376, FH = 688, NFT = 43;
constexpr int NT_ALL = 112;
constexpr size_t WS_BIMG = 0;
constexpr size_t WS_Z1 = (size_t)8 << 20;
constexpr size_t WS_H = (size_t)12 << 20;
constexpr size_t WS_Y = (size_t)16 << 20;
constexpr size_t WS_XST = (size_t)24 << 20;
constexpr size_t WS_ZST = WS_XST + 65536;
constexpr size_t WS_SALT = WS_ZST + 262144 + 65536;
constexpr size_t WS_DBG = (size_t)26 << 20;
constexpr int NPART = 2 * YSPLIT;

template <int I, int N, class F>
__device__ __forceinline__ void static_for(F&& f) {
  if constexpr (I < N) { f(std::integral_constant<int, I>{}); static_for<I + 1, N>(f); }
}
__device__ __forceinline__ unsigned pack2(float a, float b) {
  return __builtin_bit_cast(unsigned, __builtin_convertvector(f32x2{a, b}, bf16v2));
}
__device__ __forceinline__ float bflo(unsigned u) { return __uint_as_float(u << 16); }
__device__ __forceinline__ float bfhi(unsigned u) { return __uint_as_float(u & 0xffff0000u); }
__device__ __forceinline__ void glds16(const char* g, char* l) {
  __builtin_amdgcn_global_load_lds((gptr_t)g, (lptr_t)l, 16, 0, 0);
}
template <int AUX = 0>
__device__ __forceinline__ void glds16o(const char* ubase, unsigned off, char* l) {
  asm volatile("" : "+v"(off));
  __builtin_amdgcn_global_load_lds((gptr_t)(ubase + off), (lptr_t)l, 16, 0, AUX);
}
__device__ __forceinline__ void swap16(unsigned& a, unsigned& b) {
  auto r = __builtin_amdgcn_permlane16_swap(a, b, false, false);
  a = r[0]; b = r[1];
}
template <int CTRL>
__device__ __forceinline__ float dpp_add(float v) {
  return v + __int_as_float(__builtin_amdgcn_update_dpp(0, __float_as_int(v), CTRL, 0xf, 0xf, true));
}
__device__ __forceinline__ float row16_sum(float v) {
  v = dpp_add<0x128>(v); v = dpp_add<0x124>(v); v = dpp_add<0x122>(v); v = dpp_add<0x121>(v);
  return v;
}
__device__ __forceinline__ float wave_sum(float v) {
  v = row16_sum(v);
  const int i = __float_as_int(v);
  return __int_as_float(__builtin_amdgcn_readlane(i, 0)) + __int_as_float(__builtin_amdgcn_readlane(i, 16)) +
         __int_as_float(__builtin_amdgcn_readlane(i, 32)) + __int_as_float(__builtin_amdgcn_readlane(i, 48));
}
__device__ __forceinline__ float gelu_tanh(float v) {
  const float c0 = -2.3022081986f, c1 = -0.1029432397f;
  const float e = __builtin_amdgcn_exp2f(v * __builtin_fmaf(v * v, c1, c0));
  return v * __builtin_amdgcn_rcpf(1.0f + e);
}
__device__ __forceinline__ float silu(float v) {
  const float e = __builtin_amdgcn_exp2f(v * -1.4426950409f);
  return v * __builtin_amdgcn_rcpf(1.0f + e);
}
__device__ __forceinline__ bf16x8 lds_frag(const char* p) { return *(const bf16x8*)p; }
__device__ __forceinline__ bf16x4 lds_tr(const char* p) {
  return __builtin_bit_cast(bf16x4, __builtin_amdgcn_ds_read_tr16_b64_v4i16((__attribute__((address_space(3))) v4i16_t*)p));
}
__device__ __forceinline__ bf16x8 cat(bf16x4 a, bf16x4 b) { return __builtin_shufflevector(a, b, 0, 1, 2, 3, 4, 5, 6, 7); }

constexpr int AUX_SC1 = 16;
constexpr int RSRC_W3 = 0x00020000;
__device__ __forceinline__ void granule_store(__amdgpu_buffer_rsrc_t rs, int off, float v0, float v1, u32x2 tag) {
  u32x4 g = {__float_as_uint(v0), tag[0], __float_as_uint(v1), tag[1]};
  __builtin_amdgcn_raw_buffer_store_b128(g, rs, off, 0, AUX_SC1);
}
constexpr unsigned SPIN_MAX = 1u << 18;

#define TSTAMP(k, slot) do {} while (0)
#define TSTAMPW(k, slot, wv) do {} while (0)

__device__ __forceinline__ int part_start(int p) { return p < 3 ? 6 * p : 18 + 5 * (p - 3); }
__device__ __forceinline__ int part_cnt(int p) { return p < 3 ? 6 : 5; }

constexpr int A_STAGE = 36864, A_NS = 4, A_XO = 0, A_DO = 16384, A_WO = 24576;
constexpr int A_EX = A_NS * A_STAGE;
constexpr int A_CS = A_EX;
constexpr int A_XRS = A_CS + 2560;
constexpr int A_XNM = A_XRS + 512;
constexpr int A_ZPS = A_XNM + 512;
constexpr int A_ZPQ = A_ZPS + 1024;
constexpr int A_ZRS = A_ZPQ + 1024;
constexpr int A_ZNM = A_ZRS + 512;
constexpr int A_LDS = A_ZNM + 512;

template <bool Z2>
__device__ __forceinline__ void phaseA(const char* __restrict__ x, const char* __restrict__ wd, const char* __restrict__ win,
                                       char* __restrict__ ws, char* shm, int panel, int j) {
  const int tid = threadIdx.x, w = __builtin_amdgcn_readfirstlane(tid >> 6), lane = tid & 63;
  const int wr = w >> 1, wc = w & 1, c = lane & 15, q = lane >> 4;
  const int part = j & 7, fstart = part_start(part), fcnt = part_cnt(part);

  u32x2 salt; u32x4 xr;
  {
    const char* ps = ws + WS_SALT + panel * 128;
    const char* pr = x + (size_t)(panel * 128 + 8 * j + w) * 1024 + lane * 16;
    asm volatile("global_load_dwordx2 %0, %1, off sc1" : "=&v"(salt) : "v"(ps) : "memory");
    asm volatile("global_load_dwordx4 %0, %1, off" : "=&v"(xr) : "v"(pr) : "memory");
  }
  unsigned ox[2], od, ow[2];
#pragma unroll
  for (int i = 0; i < 2; ++i) {
    const int row = 8 * (w + 8 * i) + (lane >> 3);
    ox[i] = (unsigned)(panel * 128 + row) * 1024u + (unsigned)(((lane & 7) ^ ((row >> 1) & 7)) << 4);
  }
  {
    const int row = 8 * w + (lane >> 3);
    od = (unsigned)((j & 7) * 64 + row) * 1024u + (unsigned)(((lane & 7) ^ ((row >> 1) & 7)) << 4);
  }
#pragma unroll
  for (int i = 0; i < 2; ++i) {
    const int P = (i == 0) ? w : 8 + (w & 3);
    const int o = P * 1024 + lane * 16, row = o / 192, ch = (o % 192) >> 4;
    const int u = (ch >> 1) ^ ((row >> 3) & 1);
    const int ft = fstart + (u < fcnt ? u : fcnt - 1);
    ow[i] = (unsigned)row * 2752u + (unsigned)((Z2 ? FH * 2 : 0) + ft * 32 + (ch & 1) * 16);
  }
  auto issue = [&](int t) {
    char* st = shm + (t % A_NS) * A_STAGE;
    const char* xb = x + t * 128;
    const char* db = wd + t * 128;
    const char* wb = win + (size_t)t * (64 * 2752);
    asm volatile("" : "+s"(xb), "+s"(db), "+s"(wb));
    glds16o(xb, ox[0], st + A_XO + w * 1024);
    glds16o(xb, ox[1], st + A_XO + (w + 8) * 1024);
    glds16o(db, od, st + A_DO + w * 1024);
    glds16o(wb, ow[0], st + A_WO + w * 1024);
    if (w < 4) glds16o(wb, ow[1], st + A_WO + (8 + w) * 1024);
  };
#pragma unroll
  for (int t = 0; t < A_NS; ++t) issue(t);

  asm volatile("s_waitcnt vmcnt(16)" : "+v"(salt), "+v"(xr)::"memory");
  const unsigned long long tag64 = (((unsigned long long)salt[1] << 32) | salt[0]) + 0x9E3779B97F4A7C15ull;
  const u32x2 tag = {(unsigned)tag64, (unsigned)(tag64 >> 32)};
  const __amdgpu_buffer_rsrc_t rsx = __builtin_amdgcn_make_buffer_rsrc((void*)(ws + WS_XST), 0, 65536 + 262144, RSRC_W3);
  {
    float v[8];
#pragma unroll
    for (int i = 0; i < 4; ++i) { v[2 * i] = bflo(xr[i]); v[2 * i + 1] = bfhi(xr[i]); }
    float s = ((v[0] + v[1]) + (v[2] + v[3])) + ((v[4] + v[5]) + (v[6] + v[7]));
    const float mean = wave_sum(s) * (1.0f / D_);
    float ss = 0.f;
#pragma unroll
    for (int i = 0; i < 8; ++i) { const float d = v[i] - mean; ss = __builtin_fmaf(d, d, ss); }
    const float var = wave_sum(ss) * (1.0f / D_);
    const float rs = __builtin_amdgcn_rsqf(var + 1e-5f);
    if (lane == 0) granule_store(rsx, (panel * 128 + 8 * j + w) * 16, rs, -mean * rs, tag);
  }

  f32x4 acc[2][5] = {};
  f32x4 cs[5] = {};
  bf16x8 fx[2][2], fd[2][2], fw[2][3];
  const bf16x8 ones = {0x3F80, 0x3F80, 0x3F80, 0x3F80, 0x3F80, 0x3F80, 0x3F80, 0x3F80};
  int ax[2], ad[2], aw[3];
#pragma unroll
  for (int ks = 0; ks < 2; ++ks) {
    const int sw = ((ks * 4 + q) ^ ((c >> 1) & 7)) << 4;
    ax[ks] = A_XO + (32 * wr + c) * 128 + sw;
    ad[ks] = A_DO + (32 * wc + c) * 128 + sw;
  }
#pragma unroll
  for (int i = 0; i < 3; ++i) aw[i] = A_WO + (8 * q + (c >> 2)) * 192 + (((3 * wc + i) ^ (q & 1)) << 5) + (c & 3) * 8;
  auto rd = [&](int set, int t, int ks) {
    const char* st = shm + (t % A_NS) * A_STAGE;
#pragma unroll
    for (int m = 0; m < 2; ++m) fx[set][m] = lds_frag(st + ax[ks] + m * 2048);
#pragma unroll
    for (int i = 0; i < 2; ++i) fd[set][i] = lds_frag(st + ad[ks] + i * 2048);
#pragma unroll
    for (int i = 0; i < 3; ++i) fw[set][i] = cat(lds_tr(st + aw[i] + ks * 6144), lds_tr(st + aw[i] + ks * 6144 + 768));
  };
  auto mm = [&](int set, int kstep) {
#pragma unroll
    for (int m = 0; m < 2; ++m) {
#pragma unroll
      for (int i = 0; i < 2; ++i) acc[m][i] = MFMA(fx[set][m], fd[set][i], acc[m][i]);
#pragma unroll
      for (int i = 0; i < 3; ++i)
        acc[m][2 + i] = Z2 ? MFMA(fx[set][m], fw[set][i], acc[m][2 + i]) : MFMA(fw[set][i], fx[set][m], acc[m][2 + i]);
    }
    if ((kstep & 3) == wr) {
#pragma unroll
      for (int i = 0; i < 2; ++i) cs[i] = MFMA(ones, fd[set][i], cs[i]);
#pragma unroll
      for (int i = 0; i < 3; ++i) cs[2 + i] = Z2 ? MFMA(ones, fw[set][i], cs[2 + i]) : MFMA(fw[set][i], ones, cs[2 + i]);
    }
  };
  TSTAMP(0, 2);
  WAIT_V(13);
  BAR();
  TSTAMP(0, 3);
  rd(0, 0, 0);
  static_for<0, 8>([&](auto T) {
    constexpr int t = T.value;
    rd(1, t, 1);
    mm(0, 2 * t);
    WAIT_L0();
    if constexpr (t < 7) {
      constexpr int rem = (t + 3 < 7 ? t + 3 : 7) - (t + 1);
      WAIT_V(rem * 4);
    }
    BAR();
    if constexpr (t + A_NS < 8) issue(t + A_NS);
    if constexpr (t < 7) rd(0, t + 1, 0);
    mm(1, 2 * t + 1);
  });

  TSTAMP(0, 4);
  float* csum = (float*)(shm + A_CS);
  float* xrs = (float*)(shm + A_XRS);
  float* xnm = (float*)(shm + A_XNM);
#pragma unroll
  for (int i = 0; i < 5; ++i) {
    if (i < 2 || Z2) { if (q == 0) csum[((wc * 5 + i) * 16 + c) * 4 + wr] = cs[i][0]; }
    else if (c == 0) {
#pragma unroll
      for (int r = 0; r < 4; ++r) csum[((wc * 5 + i) * 16 + 4 * q + r) * 4 + wr] = cs[i][r];
    }
  }
  if (w < 2) {
    for (unsigned spin = 0; spin < SPIN_MAX; ++spin) {
      const u32x4 g = __builtin_amdgcn_raw_buffer_load_b128(rsx, (panel * 128 + tid) * 16, 0, AUX_SC1);
      const bool ok = (g[1] == tag[0]) & (g[3] == tag[1]);
      xrs[tid] = __uint_as_float(g[0]); xnm[tid] = __uint_as_float(g[2]);
      if (__all(ok)) break;
      __builtin_amdgcn_s_sleep(1);
    }
  }
  __syncthreads();
  TSTAMP(0, 5);

  char* bimg = ws + WS_BIMG + (size_t)(panel * 4 + wr) * (NT_ALL * 1024) + (c + 16 * (((q & 1) << 1) | (q >> 1))) * 16;
  f32x4 rs4[2], nm4[2];
#pragma unroll
  for (int m = 0; m < 2; ++m) {
    rs4[m] = *(const f32x4*)(xrs + 32 * wr + 16 * m + 4 * q);
    nm4[m] = *(const f32x4*)(xnm + 32 * wr + 16 * m + 4 * q);
  }
  auto colsum1 = [&](int i) { const f32x4 t = *(const f32x4*)(csum + ((wc * 5 + i) * 16 + c) * 4); return (t[0] + t[1]) + (t[2] + t[3]); };
  auto store_bt = [&](const f32x4& v0, const f32x4& v1, int ntile) {
    unsigned a0 = pack2(v0[0], v0[1]), a1 = pack2(v0[2], v0[3]), b0 = pack2(v1[0], v1[1]), b1 = pack2(v1[2], v1[3]);
    swap16(a0, b0); swap16(a1, b1);
    *(u32x4*)(bimg + ntile * 1024) = u32x4{a0, a1, b0, b1};
  };
  auto epi_ab = [&]() {
#pragma unroll
    for (int i = 0; i < 2; ++i) {
      const float cv = colsum1(i);
      store_bt(rs4[0] * acc[0][i] + nm4[0] * cv, rs4[1] * acc[1][i] + nm4[1] * cv, (j >> 3) * 32 + (j & 7) * 4 + 2 * wc + i);
    }
  };

  if constexpr (!Z2) {
    char* z1 = ws + WS_Z1 + (size_t)(panel * 4 + wr) * (NFT * 1024) + lane * 16;
    float rl[2], nl[2];
#pragma unroll
    for (int m = 0; m < 2; ++m) { rl[m] = xrs[32 * wr + 16 * m + c]; nl[m] = xnm[32 * wr + 16 * m + c]; }
#pragma unroll
    for (int i = 0; i < 3; ++i) {
      if (3 * wc + i < fcnt) {
        f32x4 cv;
#pragma unroll
        for (int r = 0; r < 4; ++r) { const f32x4 t = *(const f32x4*)(csum + ((wc * 5 + 2 + i) * 16 + 4 * q + r) * 4); cv[r] = (t[0] + t[1]) + (t[2] + t[3]); }
        unsigned p[4];
#pragma unroll
        for (int m = 0; m < 2; ++m) {
          const f32x4 v = rl[m] * acc[m][2 + i] + nl[m] * cv;
          p[2 * m] = pack2(gelu_tanh(v[0]), gelu_tanh(v[1]));
          p[2 * m + 1] = pack2(gelu_tanh(v[2]), gelu_tanh(v[3]));
        }
        *(u32x4*)(z1 + (fstart + 3 * wc + i) * 1024) = u32x4{p[0], p[1], p[2], p[3]};
      }
    }
    epi_ab();
  } else {
    f32x4 zv[2][3];
    f32x4 ssum[2] = {}, ssq[2] = {};
#pragma unroll
    for (int i = 0; i < 3; ++i) {
      const float cv = colsum1(2 + i);
      const float valid = (3 * wc + i < fcnt) ? 1.0f : 0.0f;
#pragma unroll
      for (int m = 0; m < 2; ++m) {
        const f32x4 v = rs4[m] * acc[m][2 + i] + nm4[m] * cv;
#pragma unroll
        for (int r = 0; r < 4; ++r) zv[m][i][r] = gelu_tanh(v[r]) * valid;
        ssum[m] += zv[m][i];
        ssq[m] += zv[m][i] * zv[m][i];
      }
    }
    float* zps = (float*)(shm + A_ZPS);
    float* zpq = (float*)(shm + A_ZPQ);
    float* zrs = (float*)(shm + A_ZRS);
    float* znm = (float*)(shm + A_ZNM);
#pragma unroll
    for (int m = 0; m < 2; ++m) {
#pragma unroll
      for (int r = 0; r < 4; ++r) { ssum[m][r] = row16_sum(ssum[m][r]); ssq[m][r] = row16_sum(ssq[m][r]); }
      if (c == 0) {
        *(f32x4*)(zps + wc * 128 + 32 * wr + 16 * m + 4 * q) = ssum[m];
        *(f32x4*)(zpq + wc * 128 + 32 * wr + 16 * m + 4 * q) = ssq[m];
      }
    }
    __syncthreads();
    TSTAMP(0, 6);
    const int zoff = 65536 + (panel * 8 * 128 + tid) * 16;
    if (w < 2) granule_store(rsx, zoff + part * 2048, zps[tid] + zps[128 + tid], zpq[tid] + zpq[128 + tid], tag);
    epi_ab();
    if (w < 2) {
      for (unsigned spin = 0; spin < SPIN_MAX; ++spin) {
        bool ok = true; float s = 0.f, sq = 0.f;
#pragma unroll
        for (int p = 0; p < 8; ++p) {
          const u32x4 g = __builtin_amdgcn_raw_buffer_load_b128(rsx, zoff + p * 2048, 0, AUX_SC1);
          ok = ok & (g[1] == tag[0]) & (g[3] == tag[1]);
          s += __uint_as_float(g[0]); sq += __uint_as_float(g[2]);
        }
        const float mean = s * (1.0f / FH);
        const float var = __builtin_fmaxf(sq * (1.0f / FH) - mean * mean, 0.0f);
        const float rs = __builtin_amdgcn_rsqf(var + 1e-5f);
        zrs[tid] = rs; znm[tid] = -mean * rs;
        if (__all(ok)) break;
        __builtin_amdgcn_s_sleep(1);
      }
    }
    __syncthreads();
    TSTAMP(0, 7);
    if (j == 0 && tid == 0)
      __hip_atomic_store((unsigned long long*)(ws + WS_SALT + panel * 128), tag64, __ATOMIC_RELAXED, __HIP_MEMORY_SCOPE_AGENT);
    f32x4 zr[2], zn[2];
#pragma unroll
    for (int m = 0; m < 2; ++m) {
      zr[m] = *(const f32x4*)(zrs + 32 * wr + 16 * m + 4 * q);
      zn[m] = *(const f32x4*)(znm + 32 * wr + 16 * m + 4 * q);
    }
#pragma unroll
    for (int i = 0; i < 3; ++i)
      if (3 * wc + i < fcnt) store_bt(zv[0][i] * zr[0] + zn[0], zv[1][i] * zr[1] + zn[1], 64 + fstart + 3 * wc + i);
  }
}

__global__ __launch_bounds__(512, 2) void kA(const char* __restrict__ x, const char* __restrict__ wdre, const char* __restrict__ wdim,
                                             const char* __restrict__ win, char* __restrict__ ws) {
  extern __shared__ __attribute__((aligned(1024))) char shm[];
  TSTAMP(0, 0);
  const int xcd = blockIdx.x & 7, i = blockIdx.x >> 3;
  const int panel = (xcd & 1) * 8 + (i >> 2), j = (xcd >> 1) * 4 + (i & 3);
  if (j < 8) phaseA<true>(x, wdre, win, ws, shm, panel, j);
  else phaseA<false>(x, wdim, win, ws, shm, panel, j);
  TSTAMP(0, 1);
}

template <int MT, int NT, int NS, int NSTG, bool DELAY, int AUXA>
struct GemmB {
  static constexpr int ABYTES = 32 * MT * 128, BBYTES = 4 * NT * 1024, STAGE = ABYTES + BBYTES, NP = MT + NT;

  static __device__ __forceinline__ void producer(char* shm, const char* A, const char* B, int nt0, int ncnt, int pw, int lane) {
    unsigned oa[MT], ob[NT];
#pragma unroll
    for (int i = 0; i < MT; ++i) {
      const int row = (pw * MT + i) * 8 + (lane >> 3);
      oa[i] = (unsigned)row * 4096u + (unsigned)(((lane & 7) ^ ((row >> 1) & 7)) << 4);
    }
#pragma unroll
    for (int i = 0; i < NT; ++i) {
      const int n = (pw & 1) * NT + i;
      ob[i] = (unsigned)(((pw >> 1) * NT_ALL + nt0 + (n < ncnt ? n : ncnt - 1)) * 1024 + lane * 16);
    }
    auto issue = [&](int t, int slot) {
      char* st = shm + slot * STAGE;
      const char* ab = A + t * 128;
      const char* bb = B + (size_t)t * (2 * NT_ALL * 1024);
      asm volatile("" : "+s"(ab), "+s"(bb));
#pragma unroll
      for (int i = 0; i < MT; ++i) glds16o<AUXA>(ab, oa[i], st + (pw * MT + i) * 1024);
#pragma unroll
      for (int i = 0; i < NT; ++i) glds16o(bb, ob[i], st + ABYTES + (pw * NT + i) * 1024);
    };
    constexpr int PRE = DELAY ? NS - 1 : NS;
#pragma unroll
    for (int t = 0; t < PRE; ++t) issue(t, t);
    WAIT_V((PRE - 1) * NP); BAR();
    int slot = PRE % NS;
    for (int t = 0; t < NSTG - PRE; ++t) {
      WAIT_V((PRE - 2) * NP); BAR();
      issue(t + PRE, slot);
      slot = (slot + 1 == NS) ? 0 : slot + 1;
    }
    static_for<0, PRE>([&](auto I) {
      constexpr int rem = PRE - 2 - I.value;
      WAIT_V((rem > 0 ? rem : 0) * NP); BAR();
    });
  }

  static __device__ __forceinline__ void consumer(char* shm, f32x4 (&acc)[MT][NT], int wr, int wc, int lane, char* ws) {
    const int c = lane & 15, q = lane >> 4;
    bf16x8 fa[2][MT], fb[2][NT];
    int aoff[2];
#pragma unroll
    for (int ks = 0; ks < 2; ++ks) aoff[ks] = (wr * 16 * MT + c) * 128 + (((ks * 4 + q) ^ ((c >> 1) & 7)) << 4);
    const int boff = ABYTES + wc * NT * 1024 + lane * 16;
    auto rd = [&](int set, int so, int ks) {
      const char* st = shm + so;
#pragma unroll
      for (int i = 0; i < (MT > NT ? MT : NT); ++i) {
        if (i < NT) fb[set][i] = lds_frag(st + boff + ks * (2 * NT * 1024) + i * 1024);
        if (i < MT) fa[set][i] = lds_frag(st + aoff[ks] + i * 2048);
      }
    };
    auto mm = [&](int set) {
#pragma unroll
      for (int m = 0; m < MT; ++m)
#pragma unroll
        for (int n = 0; n < NT; ++n) acc[m][n] = MFMA(fb[set][n], fa[set][m], acc[m][n]);
    };
    auto interleave = [&]() {
#pragma unroll
      for (int i = 0; i < MT + NT; ++i) { __builtin_amdgcn_sched_group_barrier(0x8, 1, 0); __builtin_amdgcn_sched_group_barrier(0x100, 1, 0); }
      __builtin_amdgcn_sched_group_barrier(0x8, MT * NT - MT - NT, 0);
    };
    BAR();
    TSTAMP(1, 2);
    rd(0, 0, 0);
    __builtin_amdgcn_s_waitcnt(0xc07f); SCHED();
    int so = 0;
    for (int t = 0; t < NSTG; ++t) {
      rd(1, so, 1); mm(0); interleave(); SCHED();
      if (!DELAY) WAIT_L0();
      BAR(); SCHED();
      so = (so + STAGE == NS * STAGE) ? 0 : so + STAGE;
      rd(0, so, 0);
      mm(1); interleave(); SCHED();
    }
  }
};

typedef GemmB<8, 4, 3, 16, false, AUXY> GemmY;
typedef GemmB<4, 3, 5, 32, true, AUXG> GemmG;
constexpr int B_LDS = (GemmY::STAGE * (YSPLIT == 1 ? 4 : 3) > GemmG::STAGE * 5) ? GemmY::STAGE * (YSPLIT == 1 ? 4 : 3) : GemmG::STAGE * 5;

__global__ __launch_bounds__(512, 2) void kB(const char* __restrict__ wsre, const char* __restrict__ wsim, const char* __restrict__ wsp,
                                             const char* __restrict__ bsp, char* __restrict__ ws) {
  extern __shared__ __attribute__((aligned(1024))) char shm[];
  TSTAMP(1, 0);
  const int tid = threadIdx.x, w = __builtin_amdgcn_readfirstlane(tid >> 6), lane = tid & 63;
  const int xcd = blockIdx.x & 7, j = blockIdx.x >> 3;
  const int c = lane & 15, q = lane >> 4;
  if (xcd < 4) {
    constexpr int MT = YSPLIT == 1 ? 4 : 8;
    const int mat = xcd >> 1, eg = j & 3;
    const int row0 = YSPLIT == 1 ? ((xcd & 1) * 8 + (j >> 2)) * 128 : (j >> 2) * 256;
    const int kh = YSPLIT == 1 ? 0 : (xcd & 1);
    if (w >= 4) {
      GemmY::producer(shm, (mat ? wsim : wsre) + (size_t)row0 * 4096 + kh * 2048, ws + WS_BIMG + (size_t)kh * 32 * (NT_ALL * 1024),
                      mat * 32 + eg * 8, 8, w - 4, lane);
    } else {
      const int wr = w >> 1, wc = w & 1;
      f32x4 acc[MT][4] = {};
      GemmY::consumer(shm, acc, wr, wc, lane, ws);
      TSTAMP(1, 3);
      char* yp = ws + WS_Y + (size_t)(YSPLIT == 1 ? mat : mat * 2 + kh) * (S_ * D_ * 2) +
                 (size_t)(row0 + wr * 16 * MT + c) * 1024 + (eg * 128 + wc * 64 + 16 * (q & 1) + 8 * (q >> 1)) * 2;
#pragma unroll
      for (int m = 0; m < MT; ++m)
#pragma unroll
        for (int h = 0; h < 2; ++h) {
          const f32x4 v0 = acc[m][2 * h], v1 = acc[m][2 * h + 1];
          unsigned a0 = pack2(v0[0], v0[1]), a1 = pack2(v0[2], v0[3]), b0 = pack2(v1[0], v1[1]), b1 = pack2(v1[2], v1[3]);
          swap16(a0, b0); swap16(a1, b1);
          *(u32x4*)(yp + m * 16384 + h * 64) = u32x4{a0, a1, b0, b1};
        }
    }
  } else {
    const int panel = (xcd - 4) * 4 + (j >> 3), part = j & 7;
    const int fstart = part_start(part), fcnt = part_cnt(part);
    if (w >= 4) {
      GemmG::producer(shm, wsp + (size_t)panel * 128 * 4096, ws + WS_BIMG, 64 + fstart, fcnt, w - 4, lane);
    } else {
      const int wr = w >> 1, wc = w & 1;
      u32x4 z1[2][3]; float bs[4];
#pragma unroll
      for (int pp = 0; pp < 2; ++pp)
#pragma unroll
        for (int n = 0; n < 3; ++n) {
          const int u = 3 * wc + n, ft = fstart + (u < fcnt ? u : fcnt - 1);
          z1[pp][n] = *(const u32x4*)(ws + WS_Z1 + (size_t)((panel * 4 + wr * 2 + pp) * NFT + ft) * 1024 + lane * 16);
        }
#pragma unroll
      for (int m = 0; m < 4; ++m) bs[m] = bflo(*(const unsigned short*)(bsp + (panel * 128 + wr * 64 + m * 16 + c) * 2));
      f32x4 acc[4][3] = {};
      GemmG::consumer(shm, acc, wr, wc, lane, ws);
      TSTAMP(1, 4);
#pragma unroll
      for (int n = 0; n < 3; ++n) {
        const int u = 3 * wc + n;
        if (u < fcnt) {
          const int ft = fstart + u;
#pragma unroll
          for (int m = 0; m < 4; ++m) {
            const unsigned zl = z1[m >> 1][n][2 * (m & 1)], zh = z1[m >> 1][n][2 * (m & 1) + 1];
            const f32x4 g = acc[m][n] + bs[m];
            const u32x2 o = {pack2(bflo(zl) * g[0], bfhi(zl) * g[1]), pack2(bflo(zh) * g[2], bfhi(zh) * g[3])};
            *(u32x2*)(ws + WS_H + (size_t)((panel * 8 + wr * 4 + m) * 22 + (ft >> 1)) * 1024 + lane * 16 + (ft & 1) * 8) = o;
          }
        }
      }
    }
  }
  TSTAMP(1, 1);
}

constexpr int C_LDS = 88 * 1024;
__global__ __launch_bounds__(512, 2) void kC(const char* __restrict__ x, const char* __restrict__ wout, const char* __restrict__ ws_c,
                                             char* __restrict__ out
) {
  extern __shared__ __attribute__((aligned(1024))) char shm[];
  TSTAMP(2, 0);
  const int tid = threadIdx.x, w = __builtin_amdgcn_readfirstlane(tid >> 6), lane = tid & 63;
  const int xcd = blockIdx.x & 7, i = blockIdx.x >> 3;
  const int tb = xcd * 4 + (i >> 3), G = i & 7;
  if (w >= 4) {
    const int pw = w - 4;
    const int rr = lane >> 3;
    const unsigned lo = (unsigned)rr * 1024u + (unsigned)(G * 128 + (((lane & 7) ^ (2 * ((rr >> 1) & 3))) << 4));
#pragma unroll
    for (int ch = 0; ch < 3; ++ch)
#pragma unroll
      for (int k = 0; k < 6; ++k) { const int P = 24 * ch + 6 * pw + k; glds16o(wout + (size_t)P * 8192, lo, shm + P * 1024); }
#pragma unroll
    for (int k = 0; k < 4; ++k) { int P = 72 + 4 * pw + k; P = P < 85 ? P : 85; glds16o(wout + (size_t)P * 8192, lo, shm + P * 1024); }
    WAIT_V(16); BAR();
    WAIT_V(10); BAR();
    WAIT_V(4); BAR();
    WAIT_V(0); BAR();
  } else {
    const int c = lane & 15, q = lane >> 4;
    const char* hp = ws_c + WS_H + (size_t)(tb * 4 + w) * (22 * 1024) + lane * 16;
    bf16x8 hf[22];
#pragma unroll
    for (int kb = 0; kb < 21; ++kb) hf[kb] = *(const bf16x8*)(hp + kb * 1024);
    hf[21] = cat(*(const bf16x4*)(hp + 21 * 1024), bf16x4{0, 0, 0, 0});
    const size_t eo = (size_t)(tb * 64 + w * 16 + c) * 1024 + (G * 64 + 16 * (q & 1) + 8 * (q >> 1)) * 2;
    u32x4 xv[2], yv[NPART][2];
#pragma unroll
    for (int h = 0; h < 2; ++h) {
      xv[h] = *(const u32x4*)(x + eo + h * 64);
#pragma unroll
      for (int p = 0; p < NPART; ++p) yv[p][h] = *(const u32x4*)(ws_c + WS_Y + (size_t)p * (S_ * D_ * 2) + eo + h * 64);
    }
    int at[4];
    {
      const int r = c >> 2, sg = (2 * (q & 1) + (r >> 1)) & 3;
#pragma unroll
      for (int T = 0; T < 4; ++T) at[T] = (4 * q + r) * 128 + ((T ^ sg) << 5) + (c & 3) * 8;
    }
    f32x4 acc[4] = {};
    bf16x8 wf[3][4];
    auto rdw = [&](auto KB) {
      constexpr int kb = KB.value;
      if constexpr (kb == 0 || kb == 6 || kb == 12 || kb == 18) { BAR(); }
#pragma unroll
      for (int T = 0; T < 4; ++T) {
        const bf16x4 lo = lds_tr(shm + at[T] + kb * 4096);
        const bf16x4 hi = (kb < 21) ? lds_tr(shm + at[T] + kb * 4096 + 2048) : bf16x4{0, 0, 0, 0};
        wf[kb % 3][T] = cat(lo, hi);
      }
    };
    rdw(std::integral_constant<int, 0>{});
    TSTAMP(2, 2);
    rdw(std::integral_constant<int, 1>{});
    SCHED();
    static_for<0, 22>([&](auto KB) {
      constexpr int kb = KB.value;
      if constexpr (kb + 2 < 22) rdw(std::integral_constant<int, kb + 2>{});
#pragma unroll
      for (int T = 0; T < 4; ++T) acc[T] = MFMA(wf[kb % 3][T], hf[kb], acc[T]);
#pragma unroll
      for (int T = 0; T < 4; ++T) { __builtin_amdgcn_sched_group_barrier(0x8, 1, 0); __builtin_amdgcn_sched_group_barrier(0x100, 2, 0); }
      SCHED();
    });
    TSTAMP(2, 3);
#pragma unroll
    for (int h = 0; h < 2; ++h) {
      float o[8];
#pragma unroll
      for (int r = 0; r < 4; ++r) {
        unsigned a = __float_as_uint(acc[2 * h][r]), b = __float_as_uint(acc[2 * h + 1][r]);
        swap16(a, b);
        o[r] = __uint_as_float(a); o[4 + r] = __uint_as_float(b);
      }
      unsigned res[4];
#pragma unroll
      for (int d = 0; d < 4; ++d) {
        float yl, yh;
        if constexpr (NPART == 2) {
          yl = bflo(yv[0][h][d]) - bflo(yv[1][h][d]); yh = bfhi(yv[0][h][d]) - bfhi(yv[1][h][d]);
        } else {
          yl = (bflo(yv[0][h][d]) + bflo(yv[1][h][d])) - (bflo(yv[2][h][d]) + bflo(yv[3][h][d]));
          yh = (bfhi(yv[0][h][d]) + bfhi(yv[1][h][d])) - (bfhi(yv[2][h][d]) + bfhi(yv[3][h][d]));
        }
        res[d] = pack2(bflo(xv[h][d]) + yl * silu(o[2 * d]), bfhi(xv[h][d]) + yh * silu(o[2 * d + 1]));
      }
      *(u32x4*)(out + eo + h * 64) = u32x4{res[0], res[1], res[2], res[3]};
    }
  }
  TSTAMP(2, 1);
}

extern "C" void kernel_launch(void* const* d_in, const int* in_sizes, int n_in, void* d_out, int out_size, void* d_ws, size_t ws_size,
                              hipStream_t stream) {
  const char* x = (const char*)d_in[0];
  const char* wsre = (const char*)d_in[1];
  const char* wsim = (const char*)d_in[2];
  const char* wdre = (const char*)d_in[3];
  const char* wdim = (const char*)d_in[4];
  const char* win = (const char*)d_in[6];
  const char* wsp = (const char*)d_in[7];
  const char* bsp = (const char*)d_in[8];
  const char* wout = (const char*)d_in[9];
  char* ws = (char*)d_ws;
  (void)hipFuncSetAttribute((const void*)kA, hipFuncAttributeMaxDynamicSharedMemorySize, A_LDS);
  (void)hipFuncSetAttribute((const void*)kB, hipFuncAttributeMaxDynamicSharedMemorySize, B_LDS);
  (void)hipFuncSetAttribute((const void*)kC, hipFuncAttributeMaxDynamicSharedMemorySize, C_LDS);
  kA<<<256, 512, A_LDS, stream>>>(x, wdre, wdim, win, ws);
  kB<<<256, 512, B_LDS, stream>>>(wsre, wsim, wsp, bsp, ws);
  kC<<<256, 512, C_LDS, stream>>>(x, wout, ws, (char*)d_out);
}
```

```cpp
#include <hip/hip_runtime.h>
#include <hip/hip_bf16.h>
#include <stdint.h>
#include <type_traits>

typedef short bf16x8 __attribute__((ext_vector_type(8)));
typedef short bf16x4 __attribute__((ext_vector_type(4)));
typedef short v4i16_t __attribute__((ext_vector_type(4)));
typedef float f32x4 __attribute__((ext_vector_type(4)));
typedef float f32x2 __attribute__((ext_vector_type(2)));
typedef unsigned u32x4 __attribute__((ext_vector_type(4)));
typedef unsigned u32x2 __attribute__((ext_vector_type(2)));
typedef __bf16 bf16v2 __attribute__((ext_vector_type(2)));
typedef const __attribute__((address_space(1))) void* gptr_t;
typedef __attribute__((address_space(3))) void* lptr_t;
typedef __amdgpu_buffer_rsrc_t rsrc_t;

#define WAIT_V(n) asm volatile("s_waitcnt vmcnt(%0)" ::"n"(n) : "memory")
#define WAIT_L0() asm volatile("s_waitcnt lgkmcnt(0)" ::: "memory")
#define BAR() do { asm volatile("" ::: "memory"); __builtin_amdgcn_s_barrier(); asm volatile("" ::: "memory"); } while (0)
#define SCHED() __builtin_amdgcn_sched_barrier(0)
#define MFMA(a, b, c) __builtin_amdgcn_mfma_f32_16x16x32_bf16(a, b, c, 0, 0, 0)

constexpr int S_ = 2048, D_ = 512, F_ = 1376, FH = 688, NFT = 43;
constexpr int NT_ALL = 112;
constexpr unsigned WS_BIMG = 0;
constexpr unsigned WS_Z1 = 8u << 20;
constexpr unsigned WS_H = 12u << 20;
constexpr unsigned WS_Y = 16u << 20;
constexpr unsigned WS_XST = 24u << 20;
constexpr unsigned WS_ZST = WS_XST + 65536;
constexpr unsigned WS_FLG = WS_ZST + 262144;
constexpr unsigned WS_SALT = WS_FLG + 131072;
constexpr unsigned WS_END = 25u << 20;
constexpr unsigned WS_DBG = 26u << 20;
constexpr int FL_AB = 0, FL_Z2 = 256, FL_Z1 = 384, FL_H = 512, FL_Y = 640;

template <int I, int N, class F>
__device__ __forceinline__ void static_for(F&& f) {
  if constexpr (I < N) { f(std::integral_constant<int, I>{}); static_for<I + 1, N>(f); }
}
__device__ __forceinline__ unsigned pack2(float a, float b) {
  return __builtin_bit_cast(unsigned, __builtin_convertvector(f32x2{a, b}, bf16v2));
}
__device__ __forceinline__ float bflo(unsigned u) { return __uint_as_float(u << 16); }
__device__ __forceinline__ float bfhi(unsigned u) { return __uint_as_float(u & 0xffff0000u); }
template <int AUX = 0>
__device__ __forceinline__ void glds16o(const char* ubase, unsigned off, char* l) {
  asm volatile("" : "+v"(off));
  __builtin_amdgcn_global_load_lds((gptr_t)(ubase + off), (lptr_t)l, 16, 0, AUX);
}
__device__ __forceinline__ void swap16(unsigned& a, unsigned& b) {
  auto r = __builtin_amdgcn_permlane16_swap(a, b, false, false);
  a = r[0]; b = r[1];
}
template <int CTRL>
__device__ __forceinline__ float dpp_add(float v) {
  return v + __int_as_float(__builtin_amdgcn_update_dpp(0, __float_as_int(v), CTRL, 0xf, 0xf, true));
}
__device__ __forceinline__ float row16_sum(float v) {
  v = dpp_add<0x128>(v); v = dpp_add<0x124>(v); v = dpp_add<0x122>(v); v = dpp_add<0x121>(v);
  return v;
}
__device__ __forceinline__ float wave_sum(float v) {
  v = row16_sum(v);
  const int i = __float_as_int(v);
  return __int_as_float(__builtin_amdgcn_readlane(i, 0)) + __int_as_float(__builtin_amdgcn_readlane(i, 16)) +
         __int_as_float(__builtin_amdgcn_readlane(i, 32)) + __int_as_float(__builtin_amdgcn_readlane(i, 48));
}
__device__ __forceinline__ float gelu_tanh(float v) {
  const float c0 = -2.3022081986f, c1 = -0.1029432397f;
  const float e = __builtin_amdgcn_exp2f(v * __builtin_fmaf(v * v, c1, c0));
  return v * __builtin_amdgcn_rcpf(1.0f + e);
}
__device__ __forceinline__ float silu(float v) {
  const float e = __builtin_amdgcn_exp2f(v * -1.4426950409f);
  return v * __builtin_amdgcn_rcpf(1.0f + e);
}
__device__ __forceinline__ bf16x8 lds_frag(const char* p) { return *(const bf16x8*)p; }
__device__ __forceinline__ bf16x4 lds_tr(const char* p) {
  return __builtin_bit_cast(bf16x4, __builtin_amdgcn_ds_read_tr16_b64_v4i16((__attribute__((address_space(3))) v4i16_t*)p));
}
__device__ __forceinline__ bf16x8 cat(bf16x4 a, bf16x4 b) { return __builtin_shufflevector(a, b, 0, 1, 2, 3, 4, 5, 6, 7); }

constexpr int SC1 = 16;
__device__ __forceinline__ void st16(rsrc_t rs, unsigned voff, unsigned soff, u32x4 v) { __builtin_amdgcn_raw_buffer_store_b128(v, rs, voff, soff, SC1); }
__device__ __forceinline__ u32x4 ld16(rsrc_t rs, unsigned voff, unsigned soff) { return __builtin_amdgcn_raw_buffer_load_b128(rs, voff, soff, SC1); }
__device__ __forceinline__ void granule_store(rsrc_t rs, unsigned off, float v0, float v1, u32x2 tag) {
  st16(rs, off, 0, u32x4{__float_as_uint(v0), tag[0], __float_as_uint(v1), tag[1]});
}
constexpr unsigned SPIN_MAX = 1u << 18;
__device__ __forceinline__ void flag_set(rsrc_t rs, int idx, u32x2 tag, int lane) {
  if (lane == 0) __builtin_amdgcn_raw_buffer_store_b64(tag, rs, WS_FLG + idx * 128, 0, SC1);
}
__device__ __forceinline__ void flag_wait(rsrc_t rs, int idx, u32x2 tag) {
  for (unsigned spin = 0; spin < SPIN_MAX; ++spin) {
    const u32x2 g = __builtin_amdgcn_raw_buffer_load_b64(rs, WS_FLG + idx * 128, 0, SC1);
    if (__all((g[0] == tag[0]) & (g[1] == tag[1]))) break;
    __builtin_amdgcn_s_sleep(1);
  }
  asm volatile("" ::: "memory");
}

#define TSTAMP(slot) do {} while (0)
#define TSTAMPW(slot, wv) do {} while (0)

__device__ __forceinline__ constexpr int part_start(int p) { return p < 3 ? 6 * p : 18 + 5 * (p - 3); }
__device__ __forceinline__ constexpr int part_cnt(int p) { return p < 3 ? 6 : 5; }

constexpr int A_STAGE = 36864, A_NS = 4, A_XO = 0, A_DO = 16384, A_WO = 24576;
constexpr int L_HO = 69632;
constexpr int L_CS = A_NS * A_STAGE;
constexpr int L_XRS = L_CS + 2048;
constexpr int L_XNM = L_XRS + 512;
constexpr int L_ZPS = L_XNM + 512;
constexpr int L_ZPQ = L_ZPS + 1024;
constexpr int L_ZRS = L_ZPQ + 1024;
constexpr int L_ZNM = L_ZRS + 512;
constexpr int LDS_BYTES = L_ZNM + 512;

template <bool G>
struct PB {
  static constexpr int NA = G ? 4 : 8, NB = G ? 3 : 4, NP = NA + NB;
  static constexpr int NS = G ? 5 : 3, NSTG = G ? 32 : 16, PF = G ? 4 : 2;
  static constexpr int ABYTES = NA * 4096, BBYTES = NB * 4096, BOFF = G ? 81920 : 98304;
  static constexpr int MT = G ? 2 : 8, NT = G ? 6 : 4, BKS = BBYTES / 2;
  static constexpr int cnt(int s) { return s >= NSTG ? 0 : (s < PF ? NB : NP); }
  static constexpr int span(int s0, int s1) { return s0 > s1 ? 0 : cnt(s0) + span(s0 + 1, s1); }
};

template <bool Z2>
__device__ __forceinline__ void body(const char* __restrict__ x, const char* __restrict__ wd, const char* __restrict__ wsq,
                                     const char* __restrict__ win, const char* __restrict__ bsp, const char* __restrict__ wout,
                                     char* __restrict__ ws, char* __restrict__ out, char* shm) {
  typedef PB<Z2> P;
  const int tid = threadIdx.x, w = __builtin_amdgcn_readfirstlane(tid >> 6), lane = tid & 63;
  const int c = lane & 15, q = lane >> 4;
  const int xcd = blockIdx.x & 7, bi = blockIdx.x >> 3;
  const bool prod = w >= 4;
  const int pw = w & 3, wr = pw >> 1, wc = pw & 1;
  const int panel = (xcd & 1) * 8 + (bi >> 2), j = (xcd >> 1) * 4 + (bi & 3);
  const int part = j & 7, fstart = part_start(part), fcnt = part_cnt(part);
  const int gpanel = xcd * 4 + (bi >> 3), gpart = bi & 7, gstart = part_start(gpart), gcnt = part_cnt(gpart);
  const int ymat = (xcd >> 1) & 1, ykh = xcd & 1, yp = bi >> 2, yeg = bi & 3;
  const int tb = xcd * 4 + (bi >> 3), G = bi & 7;

  const rsrc_t rs = __builtin_amdgcn_make_buffer_rsrc((void*)ws, 0, WS_END, 0x00020000);
  u32x2 tag = {0, 0};
  f32x4 ac[2][5];
  unsigned oa[P::NA], ob[P::NB];
  const char* Ab = wsq + (Z2 ? (size_t)gpanel * 128 * 4096 : (size_t)yp * 256 * 4096 + ykh * 2048);
  const char* Bb = ws + WS_BIMG + (Z2 ? 0 : (size_t)ykh * 32 * (NT_ALL * 1024));
  auto issueBA = [&](int t, int slot) {
    const char* ab = Ab + t * 128;
    asm volatile("" : "+s"(ab));
#pragma unroll
    for (int i = 0; i < P::NA; ++i) glds16o(ab, oa[i], shm + slot * P::ABYTES + (pw * P::NA + i) * 1024);
  };
  auto issueBB = [&](int t, int slot) {
    const char* bb = Bb + (size_t)t * (2 * NT_ALL * 1024);
    asm volatile("" : "+s"(bb));
#pragma unroll
    for (int i = 0; i < P::NB; ++i) glds16o<SC1>(bb, ob[i], shm + P::BOFF + slot * P::BBYTES + (pw * P::NB + i) * 1024);
  };

  float* csum = (float*)(shm + L_CS);
  float* xrs = (float*)(shm + L_XRS);
  float* xnm = (float*)(shm + L_XNM);
  if (prod) {
    unsigned ox[4], od[2], ow[3];
#pragma unroll
    for (int i = 0; i < 4; ++i) {
      const int row = 8 * (pw * 4 + i) + (lane >> 3);
      ox[i] = (unsigned)(panel * 128 + row) * 1024u + (unsigned)(((lane & 7) ^ ((row >> 1) & 7)) << 4);
    }
#pragma unroll
    for (int i = 0; i < 2; ++i) {
      const int row = 8 * (pw * 2 + i) + (lane >> 3);
      od[i] = (unsigned)((j & 7) * 64 + row) * 1024u + (unsigned)(((lane & 7) ^ ((row >> 1) & 7)) << 4);
    }
#pragma unroll
    for (int i = 0; i < 3; ++i) {
      const int o = (pw * 3 + i) * 1024 + lane * 16, row = o / 192, ch = (o % 192) >> 4;
      const int u = (ch >> 1) ^ ((row >> 3) & 1);
      const int ft = fstart + (u < fcnt ? u : fcnt - 1);
      ow[i] = (unsigned)row * 2752u + (unsigned)((Z2 ? FH * 2 : 0) + ft * 32 + (ch & 1) * 16);
    }
    auto issue = [&](int t) {
      char* st = shm + (t % A_NS) * A_STAGE;
      const char* xb = x + t * 128;
      const char* db = wd + t * 128;
      const char* wb = win + (size_t)t * (64 * 2752);
      asm volatile("" : "+s"(xb), "+s"(db), "+s"(wb));
#pragma unroll
      for (int i = 0; i < 4; ++i) glds16o(xb, ox[i], st + A_XO + (pw * 4 + i) * 1024);
#pragma unroll
      for (int i = 0; i < 2; ++i) glds16o(db, od[i], st + A_DO + (pw * 2 + i) * 1024);
#pragma unroll
      for (int i = 0; i < 3; ++i) glds16o(wb, ow[i], st + A_WO + (pw * 3 + i) * 1024);
    };
#pragma unroll
    for (int t = 0; t < A_NS; ++t) issue(t);
#pragma unroll
    for (int i = 0; i < P::NA; ++i) {
      const int row = (pw * P::NA + i) * 8 + (lane >> 3);
      oa[i] = (unsigned)row * 4096u + (unsigned)(((lane & 7) ^ ((row >> 1) & 7)) << 4);
    }
#pragma unroll
    for (int i = 0; i < P::NB; ++i) {
      const int n = (pw & 1) * P::NB + i;
      const int nt = Z2 ? 64 + gstart + (n < gcnt ? n : gcnt - 1) : ymat * 32 + yeg * 8 + n;
      ob[i] = (unsigned)(((pw >> 1) * NT_ALL + nt) * 1024 + lane * 16);
    }
    WAIT_V(27); BAR();
    static_for<0, 7>([&](auto T) {
      constexpr int t = T.value;
      WAIT_V(t <= 4 ? 18 : (t == 5 ? 9 : P::PF * P::NA));
      BAR();
      if constexpr (t + A_NS < 8) issue(t + A_NS);
      if constexpr (t == 5) {
#pragma unroll
        for (int s = 0; s < P::PF; ++s) issueBA(s, s);
      }
    });
  } else {
    u32x2 salt; u32x4 xr0, xr1;
    {
      const char* ps = ws + WS_SALT;
      const char* pr = x + (size_t)(panel * 128 + 8 * j + 2 * w) * 1024 + lane * 16;
      asm volatile("global_load_dwordx2 %0, %1, off sc1" : "=&v"(salt) : "v"(ps) : "memory");
      asm volatile("global_load_dwordx4 %0, %1, off" : "=&v"(xr0) : "v"(pr) : "memory");
      asm volatile("global_load_dwordx4 %0, %1, off offset:1024" : "=&v"(xr1) : "v"(pr) : "memory");
    }
    f32x4 acc[4][5] = {};
    f32x4 cs[5] = {};
    bf16x8 fx[2][4], fd[2][2], fw[2][3];
    const bf16x8 ones = {0x3F80, 0x3F80, 0x3F80, 0x3F80, 0x3F80, 0x3F80, 0x3F80, 0x3F80};
    int ax[2], ad[2], aw[3];
#pragma unroll
    for (int ks = 0; ks < 2; ++ks) {
      const int sw = ((ks * 4 + q) ^ ((c >> 1) & 7)) << 4;
      ax[ks] = A_XO + (64 * wr + c) * 128 + sw;
      ad[ks] = A_DO + (32 * wc + c) * 128 + sw;
    }
#pragma unroll
    for (int i = 0; i < 3; ++i) aw[i] = A_WO + (8 * q + (c >> 2)) * 192 + (((3 * wc + i) ^ (q & 1)) << 5) + (c & 3) * 8;
    asm volatile("s_waitcnt vmcnt(0)" : "+v"(salt), "+v"(xr0), "+v"(xr1)::"memory");
    {
      const unsigned long long tag64 = (((unsigned long long)salt[1] << 32) | salt[0]) + 0x9E3779B97F4A7C15ull;
      tag = u32x2{(unsigned)tag64, (unsigned)(tag64 >> 32)};
      float v[2][8], mean[2], ss[2];
#pragma unroll
      for (int i = 0; i < 4; ++i) { v[0][2 * i] = bflo(xr0[i]); v[0][2 * i + 1] = bfhi(xr0[i]); v[1][2 * i] = bflo(xr1[i]); v[1][2 * i + 1] = bfhi(xr1[i]); }
#pragma unroll
      for (int r = 0; r < 2; ++r)
        mean[r] = wave_sum(((v[r][0] + v[r][1]) + (v[r][2] + v[r][3])) + ((v[r][4] + v[r][5]) + (v[r][6] + v[r][7]))) * (1.0f / D_);
#pragma unroll
      for (int r = 0; r < 2; ++r) {
        ss[r] = 0.f;
#pragma unroll
        for (int i = 0; i < 8; ++i) { const float d = v[r][i] - mean[r]; ss[r] = __builtin_fmaf(d, d, ss[r]); }
        ss[r] = __builtin_amdgcn_rsqf(wave_sum(ss[r]) * (1.0f / D_) + 1e-5f);
      }
      const int r = lane & 1;
      const float rsv = r ? ss[1] : ss[0], mv = r ? mean[1] : mean[0];
      if (lane < 2) granule_store(rs, WS_XST + (panel * 128 + 8 * j + 2 * w + r) * 16, rsv, -mv * rsv, tag);
    }
    auto rd = [&](int set, int t, int ks) {
      const char* st = shm + (t % A_NS) * A_STAGE;
#pragma unroll
      for (int i = 0; i < 2; ++i) fd[set][i] = lds_frag(st + ad[ks] + i * 2048);
#pragma unroll
      for (int m = 0; m < 4; ++m) fx[set][m] = lds_frag(st + ax[ks] + m * 2048);
#pragma unroll
      for (int i = 0; i < 3; ++i) fw[set][i] = cat(lds_tr(st + aw[i] + ks * 6144), lds_tr(st + aw[i] + ks * 6144 + 768));
    };
    auto mm = [&](int set) {
#pragma unroll
      for (int i = 0; i < 2; ++i)
#pragma unroll
        for (int m = 0; m < 4; ++m) acc[m][i] = MFMA(fx[set][m], fd[set][i], acc[m][i]);
#pragma unroll
      for (int i = 0; i < 3; ++i)
#pragma unroll
        for (int m = 0; m < 4; ++m)
          acc[m][2 + i] = Z2 ? MFMA(fx[set][m], fw[set][i], acc[m][2 + i]) : MFMA(fw[set][i], fx[set][m], acc[m][2 + i]);
    };
    auto mmcs = [&](int set) {
#pragma unroll
      for (int i = 0; i < 2; ++i) cs[i] = MFMA(ones, fd[set][i], cs[i]);
#pragma unroll
      for (int i = 0; i < 3; ++i) cs[2 + i] = Z2 ? MFMA(ones, fw[set][i], cs[2 + i]) : MFMA(fw[set][i], ones, cs[2 + i]);
    };
    auto interleave = [&]() {
#pragma unroll
      for (int i = 0; i < 12; ++i) { __builtin_amdgcn_sched_group_barrier(0x8, 1, 0); __builtin_amdgcn_sched_group_barrier(0x100, 1, 0); }
      __builtin_amdgcn_sched_group_barrier(0x8, 8, 0);
    };
    const unsigned gxo = WS_XST + (panel * 128 + 32 * w + (lane & 31)) * 16;
    u32x4 gx;
    BAR();
    TSTAMP(2);
    rd(0, 0, 0);
    WAIT_L0(); SCHED();
    static_for<0, 8>([&](auto T) {
      constexpr int t = T.value;
      rd(1, t, 1); mm(0); interleave(); SCHED();
      if (wr == 0) mmcs(0);
      if constexpr (t < 7) {
        WAIT_L0(); BAR(); SCHED();
        if constexpr (t == 5) gx = ld16(rs, gxo, 0);
        rd(0, t + 1, 0); mm(1); interleave(); SCHED();
      } else {
        mm(1);
      }
      if (wr == 1) mmcs(1);
    });
    TSTAMP(3);
#pragma unroll
    for (int m = 0; m < 2; ++m)
#pragma unroll
      for (int i = 0; i < 5; ++i) {
        *(f32x4*)(shm + L_HO + (pw * 10 + m * 5 + i) * 1024 + lane * 16) = acc[2 + m][i];
        ac[m][i] = acc[m][i];
      }
#pragma unroll
    for (int i = 0; i < 5; ++i) {
      if (i < 2 || Z2) { if (q == 0) csum[((wc * 5 + i) * 16 + c) * 2 + wr] = cs[i][0]; }
      else if (c == 0) {
#pragma unroll
        for (int r = 0; r < 4; ++r) csum[((wc * 5 + i) * 16 + 4 * q + r) * 2 + wr] = cs[i][r];
      }
    }
    for (unsigned spin = 0; spin < SPIN_MAX; ++spin) {
      if (__all((gx[1] == tag[0]) & (gx[3] == tag[1]))) break;
      __builtin_amdgcn_s_sleep(1);
      gx = ld16(rs, gxo, 0);
    }
    if (lane < 32) { xrs[32 * w + lane] = __uint_as_float(gx[0]); xnm[32 * w + lane] = __uint_as_float(gx[2]); }
    WAIT_L0();
  }
  BAR();
  TSTAMP(4);

  {
    const int wr4 = 2 * wr + (w >> 2);
    if (prod) {
#pragma unroll
      for (int m = 0; m < 2; ++m)
#pragma unroll
        for (int i = 0; i < 5; ++i) ac[m][i] = *(const f32x4*)(shm + L_HO + (pw * 10 + m * 5 + i) * 1024 + lane * 16);
    }
    const unsigned bimg = WS_BIMG + (unsigned)(panel * 4 + wr4) * (NT_ALL * 1024) + (c + 16 * (((q & 1) << 1) | (q >> 1))) * 16;
    f32x4 rs4[2], nm4[2];
#pragma unroll
    for (int m = 0; m < 2; ++m) {
      rs4[m] = *(const f32x4*)(xrs + 32 * wr4 + 16 * m + 4 * q);
      nm4[m] = *(const f32x4*)(xnm + 32 * wr4 + 16 * m + 4 * q);
    }
    auto colsum1 = [&](int i) { const f32x2 t = *(const f32x2*)(csum + ((wc * 5 + i) * 16 + c) * 2); return t[0] + t[1]; };
    auto store_bt = [&](const f32x4& v0, const f32x4& v1, int ntile) {
      unsigned a0 = pack2(v0[0], v0[1]), a1 = pack2(v0[2], v0[3]), b0 = pack2(v1[0], v1[1]), b1 = pack2(v1[2], v1[3]);
      swap16(a0, b0); swap16(a1, b1);
      st16(rs, bimg + ntile * 1024, 0, u32x4{a0, a1, b0, b1});
    };
#pragma unroll
    for (int i = 0; i < 2; ++i) {
      const float cv = colsum1(i);
      store_bt(rs4[0] * ac[0][i] + nm4[0] * cv, rs4[1] * ac[1][i] + nm4[1] * cv, (j >> 3) * 32 + (j & 7) * 4 + 2 * wc + i);
    }

    if constexpr (!Z2) {
      float rl[2], nl[2];
#pragma unroll
      for (int m = 0; m < 2; ++m) { rl[m] = xrs[32 * wr4 + 16 * m + c]; nl[m] = xnm[32 * wr4 + 16 * m + c]; }
      u32x4 zp[3];
#pragma unroll
      for (int i = 0; i < 3; ++i) {
        f32x4 cv;
#pragma unroll
        for (int r = 0; r < 4; ++r) { const f32x2 t = *(const f32x2*)(csum + ((wc * 5 + 2 + i) * 16 + 4 * q + r) * 2); cv[r] = t[0] + t[1]; }
#pragma unroll
        for (int m = 0; m < 2; ++m) {
          const f32x4 v = rl[m] * ac[m][2 + i] + nl[m] * cv;
          zp[i][2 * m] = pack2(gelu_tanh(v[0]), gelu_tanh(v[1]));
          zp[i][2 * m + 1] = pack2(gelu_tanh(v[2]), gelu_tanh(v[3]));
        }
      }
      asm volatile("" : "+v"(zp[0]), "+v"(zp[1]), "+v"(zp[2]));
      WAIT_V(0); BAR();
      TSTAMP(5);
      if (w == 0) flag_set(rs, FL_AB + panel * 16 + j, tag, lane);
      const unsigned z1o = WS_Z1 + (unsigned)(panel * 4 + wr4) * (NFT * 1024) + lane * 16;
#pragma unroll
      for (int i = 0; i < 3; ++i)
        if (3 * wc + i < fcnt) st16(rs, z1o + (fstart + 3 * wc + i) * 1024, 0, zp[i]);
      if (w == 0) flag_wait(rs, FL_AB + (ykh * 8 + ((lane >> 1) & 7)) * 16 + ymat * 8 + 2 * yeg + (lane & 1), tag);
      WAIT_V(0); BAR();
      TSTAMP(8);
      if (w == 1) flag_set(rs, FL_Z1 + panel * 8 + part, tag, lane);
    } else {
      f32x4 zv[2][3];
      f32x4 ssum[2] = {}, ssq[2] = {};
#pragma unroll
      for (int i = 0; i < 3; ++i) {
        const float cv = colsum1(2 + i);
        const float valid = (3 * wc + i < fcnt) ? 1.0f : 0.0f;
#pragma unroll
        for (int m = 0; m < 2; ++m) {
          const f32x4 v = rs4[m] * ac[m][2 + i] + nm4[m] * cv;
#pragma unroll
          for (int r = 0; r < 4; ++r) zv[m][i][r] = gelu_tanh(v[r]) * valid;
          ssum[m] += zv[m][i];
          ssq[m] += zv[m][i] * zv[m][i];
        }
      }
      float* zps = (float*)(shm + L_ZPS);
      float* zpq = (float*)(shm + L_ZPQ);
      float* zrs = (float*)(shm + L_ZRS);
      float* znm = (float*)(shm + L_ZNM);
#pragma unroll
      for (int m = 0; m < 2; ++m) {
#pragma unroll
        for (int r = 0; r < 4; ++r) { ssum[m][r] = row16_sum(ssum[m][r]); ssq[m][r] = row16_sum(ssq[m][r]); }
        if (c == 0) {
          *(f32x4*)(zps + wc * 128 + 32 * wr4 + 16 * m + 4 * q) = ssum[m];
          *(f32x4*)(zpq + wc * 128 + 32 * wr4 + 16 * m + 4 * q) = ssq[m];
        }
      }
      WAIT_V(0); WAIT_L0(); BAR();
      TSTAMP(5);
      if (w == 2) flag_set(rs, FL_AB + panel * 16 + j, tag, lane);
      if (w < 2) {
        const unsigned zoff = WS_ZST + (panel * 8 * 128 + tid) * 16;
        granule_store(rs, zoff + part * 2048, zps[tid] + zps[128 + tid], zpq[tid] + zpq[128 + tid], tag);
        for (unsigned spin = 0; spin < SPIN_MAX; ++spin) {
          bool ok = true; float s = 0.f, sq = 0.f;
#pragma unroll
          for (int p = 0; p < 8; ++p) {
            const u32x4 g = ld16(rs, zoff + p * 2048, 0);
            ok = ok & (g[1] == tag[0]) & (g[3] == tag[1]);
            s += __uint_as_float(g[0]); sq += __uint_as_float(g[2]);
          }
          const float mean = s * (1.0f / FH);
          const float var = __builtin_fmaxf(sq * (1.0f / FH) - mean * mean, 0.0f);
          const float rsv = __builtin_amdgcn_rsqf(var + 1e-5f);
          zrs[tid] = rsv; znm[tid] = -mean * rsv;
          if (__all(ok)) break;
          __builtin_amdgcn_s_sleep(1);
        }
      }
      WAIT_L0(); BAR();
      TSTAMP(6);
      f32x4 zr[2], zn[2];
#pragma unroll
      for (int m = 0; m < 2; ++m) {
        zr[m] = *(const f32x4*)(zrs + 32 * wr4 + 16 * m + 4 * q);
        zn[m] = *(const f32x4*)(znm + 32 * wr4 + 16 * m + 4 * q);
      }
#pragma unroll
      for (int i = 0; i < 3; ++i)
        if (3 * wc + i < fcnt) store_bt(zv[0][i] * zr[0] + zn[0], zv[1][i] * zr[1] + zn[1], 64 + fstart + 3 * wc + i);
      WAIT_V(0); BAR();
      TSTAMP(7);
      if (w == 0) {
        flag_set(rs, FL_Z2 + panel * 8 + part, tag, lane);
        flag_wait(rs, lane < 16 ? FL_Z2 + lane * 8 + gpart : FL_Z1 + gpanel * 8 + gpart, tag);
      }
      BAR();
      TSTAMP(8);
    }
  }

  if (prod) {
#pragma unroll
    for (int s = 0; s < P::PF; ++s) issueBB(s, s);
#pragma unroll
    for (int s = P::PF; s < P::NS; ++s) { issueBA(s, s); issueBB(s, s); }
    WAIT_V(P::span(1, P::NS - 1)); BAR();
    constexpr int NPEEL = P::PF > 2 ? P::PF - 2 : 0;
    static_for<0, NPEEL>([&](auto T) {
      constexpr int t = T.value;
      WAIT_V(P::span(t + 2, t + P::NS - 1)); BAR();
      issueBA(t + P::NS, t % P::NS); issueBB(t + P::NS, t % P::NS);
    });
    int slot = NPEEL % P::NS;
    for (int t = NPEEL; t < P::NSTG - P::NS; ++t) {
      WAIT_V((P::NS - 2) * P::NP); BAR();
      issueBA(t + P::NS, slot); issueBB(t + P::NS, slot);
      slot = (slot + 1 == P::NS) ? 0 : slot + 1;
    }
    static_for<0, P::NS>([&](auto I) {
      constexpr int rem = P::NS - 2 - I.value;
      WAIT_V((rem > 0 ? rem : 0) * P::NP); BAR();
    });
    const int rr = lane >> 3;
    const unsigned lo = (unsigned)rr * 1024u + (unsigned)(G * 128 + (((lane & 7) ^ (2 * ((rr >> 1) & 3))) << 4));
#pragma unroll
    for (int k = 0; k < 22; ++k) {
      if (k == 10) BAR();
      int Pc = 4 * k + pw; Pc = Pc < 85 ? Pc : 85;
      glds16o(wout + (size_t)Pc * 8192, lo, shm + Pc * 1024);
    }
    WAIT_V(0); BAR();
  } else {
    f32x4 acc[P::MT][P::NT] = {};
    u32x4 z1[6]; float bs[2];
    if constexpr (Z2) {
#pragma unroll
      for (int n = 0; n < 6; ++n)
        z1[n] = ld16(rs, WS_Z1 + (unsigned)((gpanel * 4 + w) * NFT + gstart + (n < gcnt ? n : gcnt - 1)) * 1024 + lane * 16, 0);
#pragma unroll
      for (int m = 0; m < 2; ++m) bs[m] = bflo(*(const unsigned short*)(bsp + (gpanel * 128 + w * 32 + m * 16 + c) * 2));
    }
    {
      constexpr int MT = P::MT, NT = P::NT;
      bf16x8 fa[2][MT], fb[2][NT];
      int aoff[2];
#pragma unroll
      for (int ks = 0; ks < 2; ++ks) aoff[ks] = ((Z2 ? 32 * w : 128 * wr) + c) * 128 + (((ks * 4 + q) ^ ((c >> 1) & 7)) << 4);
      const int boff = P::BOFF + (Z2 ? 0 : wc * 4096) + lane * 16;
      auto rd = [&](int set, int slot, int ks) {
        const char* sa = shm + slot * P::ABYTES;
        const char* sb = shm + slot * P::BBYTES;
#pragma unroll
        for (int i = 0; i < (MT > NT ? MT : NT); ++i) {
          if (i < NT) fb[set][i] = lds_frag(sb + boff + ks * P::BKS + i * 1024);
          if (i < MT) fa[set][i] = lds_frag(sa + aoff[ks] + i * 2048);
        }
      };
      auto mm = [&](int set) {
#pragma unroll
        for (int m = 0; m < MT; ++m)
#pragma unroll
          for (int n = 0; n < NT; ++n) acc[m][n] = MFMA(fb[set][n], fa[set][m], acc[m][n]);
      };
      auto interleave = [&]() {
#pragma unroll
        for (int i = 0; i < MT + NT; ++i) { __builtin_amdgcn_sched_group_barrier(0x8, 1, 0); __builtin_amdgcn_sched_group_barrier(0x100, 1, 0); }
        __builtin_amdgcn_sched_group_barrier(0x8, MT * NT - MT - NT, 0);
      };
      BAR();
      TSTAMP(9);
      rd(0, 0, 0);
      WAIT_L0(); SCHED();
      int slot = 0;
      for (int t = 0; t < P::NSTG; ++t) {
        rd(1, slot, 1); mm(0); interleave(); SCHED();
        WAIT_L0(); BAR(); SCHED();
        slot = (slot + 1 == P::NS) ? 0 : slot + 1;
        rd(0, slot, 0);
        mm(1); interleave(); SCHED();
      }
    }
    TSTAMP(10);
    if constexpr (Z2) {
#pragma unroll
      for (int m = 0; m < 2; ++m)
#pragma unroll
        for (int p = 0; p < 3; ++p) {
          u32x4 o;
#pragma unroll
          for (int h = 0; h < 2; ++h) {
            const int n = 2 * p + h;
            const unsigned zl = z1[n][2 * m], zh = z1[n][2 * m + 1];
            const f32x4 g = acc[m][n] + bs[m];
            o[2 * h] = pack2(bflo(zl) * g[0], bfhi(zl) * g[1]);
            o[2 * h + 1] = pack2(bflo(zh) * g[2], bfhi(zh) * g[3]);
          }
          if (p == 2 && gcnt < 6) { o[2] = 0; o[3] = 0; }
          st16(rs, WS_H + (unsigned)((gpanel * 8 + 2 * w + m) * 24 + gpart * 3 + p) * 1024 + lane * 16, 0, o);
        }
    } else {
      const unsigned yo = WS_Y + (unsigned)(((ymat * 2 + ykh) * 128 + yp * 16 + wr * 8) * 16 + yeg * 4 + wc * 2) * 1024 + lane * 16;
#pragma unroll
      for (int m = 0; m < 8; ++m)
#pragma unroll
        for (int h = 0; h < 2; ++h) {
          const f32x4 v0 = acc[m][2 * h], v1 = acc[m][2 * h + 1];
          unsigned a0 = pack2(v0[0], v0[1]), a1 = pack2(v0[2], v0[3]), b0 = pack2(v1[0], v1[1]), b1 = pack2(v1[2], v1[3]);
          swap16(a0, b0); swap16(a1, b1);
          st16(rs, yo + m * 16384 + h * 1024, 0, u32x4{a0, a1, b0, b1});
        }
    }
    WAIT_V(0); BAR();
    TSTAMP(11);
    if (w == 0) flag_set(rs, Z2 ? FL_H + gpanel * 8 + gpart : FL_Y + ((ymat * 2 + ykh) * 8 + yp) * 4 + yeg, tag, lane);
    const size_t eo = (size_t)(tb * 64 + w * 16 + c) * 1024 + (G * 64 + 16 * (q & 1) + 8 * (q >> 1)) * 2;
    u32x4 xv[2];
#pragma unroll
    for (int h = 0; h < 2; ++h) xv[h] = *(const u32x4*)(x + eo + h * 64);
    if (w == 0) flag_wait(rs, lane < 8 ? FL_H + (tb >> 1) * 8 + lane : FL_Y + (((lane - 8) & 3) * 8 + (tb >> 2)) * 4 + (G >> 1), tag);
    if (w == 1 && blockIdx.x == 0) {
#pragma unroll
      for (int k = 0; k < 4; ++k) flag_wait(rs, FL_AB + k * 64 + lane, tag);
      if (lane == 0) __builtin_amdgcn_raw_buffer_store_b64(tag, rs, WS_SALT, 0, SC1);
    }
    BAR();
    TSTAMP(12);
    bf16x8 hf[24];
    u32x4 yv[4][2];
#pragma unroll
    for (int kb = 0; kb < 24; ++kb) hf[kb] = __builtin_bit_cast(bf16x8, ld16(rs, WS_H + (unsigned)((tb * 4 + w) * 24 + kb) * 1024 + lane * 16, 0));
#pragma unroll
    for (int h = 0; h < 2; ++h)
#pragma unroll
      for (int p = 0; p < 4; ++p) yv[p][h] = ld16(rs, WS_Y + (unsigned)((p * 128 + tb * 4 + w) * 16 + 2 * G + h) * 1024 + lane * 16, 0);
    int at[4];
    {
      const int r = c >> 2, sg = (2 * (q & 1) + (r >> 1)) & 3;
#pragma unroll
      for (int T = 0; T < 4; ++T) at[T] = (4 * q + r) * 128 + ((T ^ sg) << 5) + (c & 3) * 8;
    }
    f32x4 oc[4] = {};
    bf16x8 wf[3][4];
    auto rdw = [&](auto KB) {
      constexpr int kb = KB.value, pt = kb / 3, pr = kb % 3, ro = (part_start(pt) + 2 * pr) * 2048;
      constexpr bool full = 2 * pr + 1 < part_cnt(pt);
#pragma unroll
      for (int T = 0; T < 4; ++T) {
        const bf16x4 lo = lds_tr(shm + at[T] + ro);
        const bf16x4 hi = full ? lds_tr(shm + at[T] + ro + 2048) : bf16x4{0, 0, 0, 0};
        wf[kb % 3][T] = cat(lo, hi);
      }
    };
    rdw(std::integral_constant<int, 0>{});
    rdw(std::integral_constant<int, 1>{});
    SCHED();
    static_for<0, 24>([&](auto KB) {
      constexpr int kb = KB.value;
      if constexpr (kb + 2 < 24) rdw(std::integral_constant<int, kb + 2>{});
#pragma unroll
      for (int T = 0; T < 4; ++T) oc[T] = MFMA(wf[kb % 3][T], hf[kb], oc[T]);
#pragma unroll
      for (int T = 0; T < 4; ++T) { __builtin_amdgcn_sched_group_barrier(0x8, 1, 0); __builtin_amdgcn_sched_group_barrier(0x100, 2, 0); }
      SCHED();
    });
    TSTAMP(13);
#pragma unroll
    for (int h = 0; h < 2; ++h) {
      float o[8];
#pragma unroll
      for (int r = 0; r < 4; ++r) {
        unsigned a = __float_as_uint(oc[2 * h][r]), b = __float_as_uint(oc[2 * h + 1][r]);
        swap16(a, b);
        o[r] = __uint_as_float(a); o[4 + r] = __uint_as_float(b);
      }
      unsigned res[4];
#pragma unroll
      for (int d = 0; d < 4; ++d) {
        const float yl = (bflo(yv[0][h][d]) + bflo(yv[1][h][d])) - (bflo(yv[2][h][d]) + bflo(yv[3][h][d]));
        const float yh = (bfhi(yv[0][h][d]) + bfhi(yv[1][h][d])) - (bfhi(yv[2][h][d]) + bfhi(yv[3][h][d]));
        res[d] = pack2(bflo(xv[h][d]) + yl * silu(o[2 * d]), bfhi(xv[h][d]) + yh * silu(o[2 * d + 1]));
      }
      *(u32x4*)(out + eo + h * 64) = u32x4{res[0], res[1], res[2], res[3]};
    }
  }
}

__global__ __launch_bounds__(512, 2) void kF(const char* __restrict__ x, const char* __restrict__ wsre, const char* __restrict__ wsim,
                                             const char* __restrict__ wdre, const char* __restrict__ wdim, const char* __restrict__ win,
                                             const char* __restrict__ wsp, const char* __restrict__ bsp, const char* __restrict__ wout,
                                             char* __restrict__ ws, char* __restrict__ out) {
  extern __shared__ __attribute__((aligned(1024))) char shm[];
  TSTAMP(0);
  const int xcd = blockIdx.x & 7;
  if (xcd < 4) body<true>(x, wdre, wsp, win, bsp, wout, ws, out, shm);
  else body<false>(x, wdim, (xcd & 2) ? wsim : wsre, win, bsp, wout, ws, out, shm);
  TSTAMP(1);
}

extern "C" void kernel_launch(void* const* d_in, const int* in_sizes, int n_in, void* d_out, int out_size, void* d_ws, size_t ws_size,
                              hipStream_t stream) {
  (void)hipFuncSetAttribute((const void*)kF, hipFuncAttributeMaxDynamicSharedMemorySize, LDS_BYTES);
  kF<<<256, 512, LDS_BYTES, stream>>>((const char*)d_in[0], (const char*)d_in[1], (const char*)d_in[2], (const char*)d_in[3],
                                      (const char*)d_in[4], (const char*)d_in[6], (const char*)d_in[7], (const char*)d_in[8],
                                      (const char*)d_in[9], (char*)d_ws, (char*)d_out);
}
```

```cpp
#define TIMING
#include <hip/hip_runtime.h>
#include <hip/hip_bf16.h>
#include <stdint.h>
#include <type_traits>

typedef short bf16x8 __attribute__((ext_vector_type(8)));
typedef short bf16x4 __attribute__((ext_vector_type(4)));
typedef short v4i16_t __attribute__((ext_vector_type(4)));
typedef float f32x4 __attribute__((ext_vector_type(4)));
typedef float f32x2 __attribute__((ext_vector_type(2)));
typedef unsigned u32x4 __attribute__((ext_vector_type(4)));
typedef unsigned u32x2 __attribute__((ext_vector_type(2)));
typedef __bf16 bf16v2 __attribute__((ext_vector_type(2)));
typedef const __attribute__((address_space(1))) void* gptr_t;
typedef __attribute__((address_space(3))) void* lptr_t;
typedef __amdgpu_buffer_rsrc_t rsrc_t;

#define WAIT_V(n) asm volatile("s_waitcnt vmcnt(%0)" ::"n"(n) : "memory")
#define WAIT_L0() asm volatile("s_waitcnt lgkmcnt(0)" ::: "memory")
#define BAR() do { asm volatile("" ::: "memory"); __builtin_amdgcn_s_barrier(); asm volatile("" ::: "memory"); } while (0)
#define SCHED() __builtin_amdgcn_sched_barrier(0)
#define MFMA(a, b, c) __builtin_amdgcn_mfma_f32_16x16x32_bf16(a, b, c, 0, 0, 0)

constexpr int S_ = 2048, D_ = 512, F_ = 1376, FH = 688, NFT = 43;
constexpr int NT_ALL = 112;
constexpr unsigned WS_BIMG = 0;
constexpr unsigned WS_Z1 = 8u << 20;
constexpr unsigned WS_H = 12u << 20;
constexpr unsigned WS_Y = 16u << 20;
constexpr unsigned WS_XST = 24u << 20;
constexpr unsigned WS_ZST = WS_XST + 65536;
constexpr unsigned WS_FLG = WS_ZST + 262144;
constexpr unsigned WS_SALT = WS_FLG + 131072;
constexpr unsigned WS_END = 25u << 20;
constexpr unsigned WS_DBG = 26u << 20;
constexpr int FL_AB = 0, FL_Z2 = 256, FL_Z1 = 384, FL_H = 512, FL_Y = 640;

template <int I, int N, class F>
__device__ __forceinline__ void static_for(F&& f) {
  if constexpr (I < N) { f(std::integral_constant<int, I>{}); static_for<I + 1, N>(f); }
}
__device__ __forceinline__ unsigned pack2(float a, float b) {
  return __builtin_bit_cast(unsigned, __builtin_convertvector(f32x2{a, b}, bf16v2));
}
__device__ __forceinline__ float bflo(unsigned u) { return __uint_as_float(u << 16); }
__device__ __forceinline__ float bfhi(unsigned u) { return __uint_as_float(u & 0xffff0000u); }
template <int AUX = 0>
__device__ __forceinline__ void glds16o(const char* ubase, unsigned off, char* l) {
  asm volatile("" : "+v"(off));
  __builtin_amdgcn_global_load_lds((gptr_t)(ubase + off), (lptr_t)l, 16, 0, AUX);
}
__device__ __forceinline__ void swap16(unsigned& a, unsigned& b) {
  auto r = __builtin_amdgcn_permlane16_swap(a, b, false, false);
  a = r[0]; b = r[1];
}
template <int CTRL>
__device__ __forceinline__ float dpp_add(float v) {
  return v + __int_as_float(__builtin_amdgcn_update_dpp(0, __float_as_int(v), CTRL, 0xf, 0xf, true));
}
__device__ __forceinline__ float row16_sum(float v) {
  v = dpp_add<0x128>(v); v = dpp_add<0x124>(v); v = dpp_add<0x122>(v); v = dpp_add<0x121>(v);
  return v;
}
__device__ __forceinline__ float wave_sum(float v) {
  v = row16_sum(v);
  const int i = __float_as_int(v);
  return __int_as_float(__builtin_amdgcn_readlane(i, 0)) + __int_as_float(__builtin_amdgcn_readlane(i, 16)) +
         __int_as_float(__builtin_amdgcn_readlane(i, 32)) + __int_as_float(__builtin_amdgcn_readlane(i, 48));
}
__device__ __forceinline__ float gelu_tanh(float v) {
  const float c0 = -2.3022081986f, c1 = -0.1029432397f;
  const float e = __builtin_amdgcn_exp2f(v * __builtin_fmaf(v * v, c1, c0));
  return v * __builtin_amdgcn_rcpf(1.0f + e);
}
__device__ __forceinline__ float silu(float v) {
  const float e = __builtin_amdgcn_exp2f(v * -1.4426950409f);
  return v * __builtin_amdgcn_rcpf(1.0f + e);
}
__device__ __forceinline__ bf16x8 lds_frag(const char* p) { return *(const bf16x8*)p; }
__device__ __forceinline__ bf16x4 lds_tr(const char* p) {
  return __builtin_bit_cast(bf16x4, __builtin_amdgcn_ds_read_tr16_b64_v4i16((__attribute__((address_space(3))) v4i16_t*)p));
}
__device__ __forceinline__ bf16x8 cat(bf16x4 a, bf16x4 b) { return __builtin_shufflevector(a, b, 0, 1, 2, 3, 4, 5, 6, 7); }

constexpr int SC1 = 16;
__device__ __forceinline__ void st16(rsrc_t rs, unsigned voff, unsigned soff, u32x4 v) { __builtin_amdgcn_raw_buffer_store_b128(v, rs, voff, soff, SC1); }
__device__ __forceinline__ u32x4 ld16(rsrc_t rs, unsigned voff, unsigned soff) { return __builtin_amdgcn_raw_buffer_load_b128(rs, voff, soff, SC1); }
__device__ __forceinline__ void granule_store(rsrc_t rs, unsigned off, float v0, float v1, u32x2 tag) {
  st16(rs, off, 0, u32x4{__float_as_uint(v0), tag[0], __float_as_uint(v1), tag[1]});
}
constexpr unsigned SPIN_MAX = 1u << 18;
__device__ __forceinline__ void flag_set(rsrc_t rs, int idx, u32x2 tag, int lane) {
  if (lane == 0) __builtin_amdgcn_raw_buffer_store_b64(tag, rs, WS_FLG + idx * 128, 0, SC1);
}
__device__ __forceinline__ void flag_wait(rsrc_t rs, int idx, u32x2 tag) {
  for (unsigned spin = 0; spin < SPIN_MAX; ++spin) {
    const u32x2 g = __builtin_amdgcn_raw_buffer_load_b64(rs, WS_FLG + idx * 128, 0, SC1);
    if (__all((g[0] == tag[0]) & (g[1] == tag[1]))) break;
    __builtin_amdgcn_s_sleep(1);
  }
  asm volatile("" ::: "memory");
}

#define TSTAMPW(slot, wv) do { if (__builtin_amdgcn_readfirstlane(threadIdx.x >> 6) == (wv)) { unsigned long long* tp_ = (unsigned long long*)(ws + WS_DBG) + blockIdx.x * 16 + (slot); unsigned long long tv_ = __builtin_amdgcn_s_memrealtime(); asm volatile("global_store_dwordx2 %0, %1, off" :: "v"(tp_), "v"(tv_) : "memory"); } } while (0)
#define TSTAMP(slot) TSTAMPW(slot, 1)

__device__ __forceinline__ constexpr int part_start(int p) { return p < 3 ? 6 * p : 18 + 5 * (p - 3); }
__device__ __forceinline__ constexpr int part_cnt(int p) { return p < 3 ? 6 : 5; }

constexpr int A_STAGE = 36864, A_NS = 4, A_XO = 0, A_DO = 16384, A_WO = 24576;
constexpr int L_HO = 69632;
constexpr int L_CS = A_NS * A_STAGE;
constexpr int L_XRS = L_CS + 2048;
constexpr int L_XNM = L_XRS + 512;
constexpr int L_ZPS = L_XNM + 512;
constexpr int L_ZPQ = L_ZPS + 1024;
constexpr int L_ZRS = L_ZPQ + 1024;
constexpr int L_ZNM = L_ZRS + 512;
constexpr int LDS_BYTES = L_ZNM + 512;

template <bool G>
struct PB {
  static constexpr int NA = G ? 4 : 8, NB = G ? 3 : 4, NP = NA + NB;
  static constexpr int NS = G ? 5 : 3, NSTG = G ? 32 : 16, PF = G ? 4 : 2;
  static constexpr int ABYTES = NA * 4096, BBYTES = NB * 4096, BOFF = G ? 81920 : 98304;
  static constexpr int MT = G ? 2 : 8, NT = G ? 6 : 4, BKS = BBYTES / 2;
  static constexpr int cnt(int s) { return s >= NSTG ? 0 : (s < PF ? NB : NP); }
  static constexpr int span(int s0, int s1) { return s0 > s1 ? 0 : cnt(s0) + span(s0 + 1, s1); }
};

template <bool Z2>
__device__ __forceinline__ void body(const char* __restrict__ x, const char* __restrict__ wd, const char* __restrict__ wsq,
                                     const char* __restrict__ win, const char* __restrict__ bsp, const char* __restrict__ wout,
                                     char* __restrict__ ws, char* __restrict__ out, char* shm) {
  typedef PB<Z2> P;
  const int tid = threadIdx.x, w = __builtin_amdgcn_readfirstlane(tid >> 6), lane = tid & 63;
  const int c = lane & 15, q = lane >> 4;
  const int xcd = blockIdx.x & 7, bi = blockIdx.x >> 3;
  const bool prod = w >= 4;
  const int pw = w & 3, wr = pw >> 1, wc = pw & 1;
  const int panel = (xcd & 1) * 8 + (bi >> 2), j = (xcd >> 1) * 4 + (bi & 3);
  const int part = j & 7, fstart = part_start(part), fcnt = part_cnt(part);
  const int gpanel = xcd * 4 + (bi >> 3), gpart = bi & 7, gstart = part_start(gpart), gcnt = part_cnt(gpart);
  const int ymat = (xcd >> 1) & 1, ykh = xcd & 1, yp = bi >> 2, yeg = bi & 3;
  const int tb = xcd * 4 + (bi >> 3), G = bi & 7;

  const rsrc_t rs = __builtin_amdgcn_make_buffer_rsrc((void*)ws, 0, WS_END, 0x00020000);
  u32x2 tag = {0, 0};
  f32x4 ac[2][5];
  unsigned oa[P::NA], ob[P::NB];
  const char* Ab = wsq + (Z2 ? (size_t)gpanel * 128 * 4096 : (size_t)yp * 256 * 4096 + ykh * 2048);
  const char* Bb = ws + WS_BIMG + (Z2 ? 0 : (size_t)ykh * 32 * (NT_ALL * 1024));
  auto issueBA = [&](int t, int slot) {
    const char* ab = Ab + t * 128;
    asm volatile("" : "+s"(ab));
#pragma unroll
    for (int i = 0; i < P::NA; ++i) glds16o(ab, oa[i], shm + slot * P::ABYTES + (pw * P::NA + i) * 1024);
  };
  auto issueBB = [&](int t, int slot) {
    const char* bb = Bb + (size_t)t * (2 * NT_ALL * 1024);
    asm volatile("" : "+s"(bb));
#pragma unroll
    for (int i = 0; i < P::NB; ++i) glds16o<SC1>(bb, ob[i], shm + P::BOFF + slot * P::BBYTES + (pw * P::NB + i) * 1024);
  };

  float* csum = (float*)(shm + L_CS);
  float* xrs = (float*)(shm + L_XRS);
  float* xnm = (float*)(shm + L_XNM);
  if (prod) {
    unsigned ox[4], od[2], ow[3];
#pragma unroll
    for (int i = 0; i < 4; ++i) {
      const int row = 8 * (pw * 4 + i) + (lane >> 3);
      ox[i] = (unsigned)(panel * 128 + row) * 1024u + (unsigned)(((lane & 7) ^ ((row >> 1) & 7)) << 4);
    }
#pragma unroll
    for (int i = 0; i < 2; ++i) {
      const int row = 8 * (pw * 2 + i) + (lane >> 3);
      od[i] = (unsigned)((j & 7) * 64 + row) * 1024u + (unsigned)(((lane & 7) ^ ((row >> 1) & 7)) << 4);
    }
#pragma unroll
    for (int i = 0; i < 3; ++i) {
      const int o = (pw * 3 + i) * 1024 + lane * 16, row = o / 192, ch = (o % 192) >> 4;
      const int u = (ch >> 1) ^ ((row >> 3) & 1);
      const int ft = fstart + (u < fcnt ? u : fcnt - 1);
      ow[i] = (unsigned)row * 2752u + (unsigned)((Z2 ? FH * 2 : 0) + ft * 32 + (ch & 1) * 16);
    }
    auto issue = [&](int t) {
      char* st = shm + (t % A_NS) * A_STAGE;
      const char* xb = x + t * 128;
      const char* db = wd + t * 128;
      const char* wb = win + (size_t)t * (64 * 2752);
      asm volatile("" : "+s"(xb), "+s"(db), "+s"(wb));
#pragma unroll
      for (int i = 0; i < 4; ++i) glds16o(xb, ox[i], st + A_XO + (pw * 4 + i) * 1024);
#pragma unroll
      for (int i = 0; i < 2; ++i) glds16o(db, od[i], st + A_DO + (pw * 2 + i) * 1024);
#pragma unroll
      for (int i = 0; i < 3; ++i) glds16o(wb, ow[i], st + A_WO + (pw * 3 + i) * 1024);
    };
#pragma unroll
    for (int t = 0; t < A_NS; ++t) issue(t);
#pragma unroll
    for (int i = 0; i < P::NA; ++i) {
      const int row = (pw * P::NA + i) * 8 + (lane >> 3);
      oa[i] = (unsigned)row * 4096u + (unsigned)(((lane & 7) ^ ((row >> 1) & 7)) << 4);
    }
#pragma unroll
    for (int i = 0; i < P::NB; ++i) {
      const int n = (pw & 1) * P::NB + i;
      const int nt = Z2 ? 64 + gstart + (n < gcnt ? n : gcnt - 1) : ymat * 32 + yeg * 8 + n;
      ob[i] = (unsigned)(((pw >> 1) * NT_ALL + nt) * 1024 + lane * 16);
    }
    WAIT_V(27); BAR();
    static_for<0, 7>([&](auto T) {
      constexpr int t = T.value;
      WAIT_V(t <= 4 ? 18 : (t == 5 ? 9 : P::PF * P::NA));
      BAR();
      if constexpr (t + A_NS < 8) issue(t + A_NS);
      if constexpr (t == 5) {
#pragma unroll
        for (int s = 0; s < P::PF; ++s) issueBA(s, s);
      }
    });
  } else {
    u32x2 salt; u32x4 xr0, xr1;
    {
      const char* ps = ws + WS_SALT;
      const char* pr = x + (size_t)(panel * 128 + 8 * j + 2 * w) * 1024 + lane * 16;
      asm volatile("global_load_dwordx2 %0, %1, off sc1" : "=&v"(salt) : "v"(ps) : "memory");
      asm volatile("global_load_dwordx4 %0, %1, off" : "=&v"(xr0) : "v"(pr) : "memory");
      asm volatile("global_load_dwordx4 %0, %1, off offset:1024" : "=&v"(xr1) : "v"(pr) : "memory");
    }
    f32x4 acc[4][5] = {};
    f32x4 cs[5] = {};
    bf16x8 fx[2][4], fd[2][2], fw[2][3];
    const bf16x8 ones = {0x3F80, 0x3F80, 0x3F80, 0x3F80, 0x3F80, 0x3F80, 0x3F80, 0x3F80};
    int ax[2], ad[2], aw[3];
#pragma unroll
    for (int ks = 0; ks < 2; ++ks) {
      const int sw = ((ks * 4 + q) ^ ((c >> 1) & 7)) << 4;
      ax[ks] = A_XO + (64 * wr + c) * 128 + sw;
      ad[ks] = A_DO + (32 * wc + c) * 128 + sw;
    }
#pragma unroll
    for (int i = 0; i < 3; ++i) aw[i] = A_WO + (8 * q + (c >> 2)) * 192 + (((3 * wc + i) ^ (q & 1)) << 5) + (c & 3) * 8;
    asm volatile("s_waitcnt vmcnt(0)" : "+v"(salt), "+v"(xr0), "+v"(xr1)::"memory");
    {
      const unsigned long long tag64 = (((unsigned long long)salt[1] << 32) | salt[0]) + 0x9E3779B97F4A7C15ull;
      tag = u32x2{(unsigned)tag64, (unsigned)(tag64 >> 32)};
      float v[2][8], mean[2], ss[2];
#pragma unroll
      for (int i = 0; i < 4; ++i) { v[0][2 * i] = bflo(xr0[i]); v[0][2 * i + 1] = bfhi(xr0[i]); v[1][2 * i] = bflo(xr1[i]); v[1][2 * i + 1] = bfhi(xr1[i]); }
#pragma unroll
      for (int r = 0; r < 2; ++r)
        mean[r] = wave_sum(((v[r][0] + v[r][1]) + (v[r][2] + v[r][3])) + ((v[r][4] + v[r][5]) + (v[r][6] + v[r][7]))) * (1.0f / D_);
#pragma unroll
      for (int r = 0; r < 2; ++r) {
        ss[r] = 0.f;
#pragma unroll
        for (int i = 0; i < 8; ++i) { const float d = v[r][i] - mean[r]; ss[r] = __builtin_fmaf(d, d, ss[r]); }
        ss[r] = __builtin_amdgcn_rsqf(wave_sum(ss[r]) * (1.0f / D_) + 1e-5f);
      }
      const int r = lane & 1;
      const float rsv = r ? ss[1] : ss[0], mv = r ? mean[1] : mean[0];
      if (lane < 2) granule_store(rs, WS_XST + (panel * 128 + 8 * j + 2 * w + r) * 16, rsv, -mv * rsv, tag);
    }
    auto rd = [&](int set, int t, int ks) {
      const char* st = shm + (t % A_NS) * A_STAGE;
#pragma unroll
      for (int i = 0; i < 2; ++i) fd[set][i] = lds_frag(st + ad[ks] + i * 2048);
#pragma unroll
      for (int m = 0; m < 4; ++m) fx[set][m] = lds_frag(st + ax[ks] + m * 2048);
#pragma unroll
      for (int i = 0; i < 3; ++i) fw[set][i] = cat(lds_tr(st + aw[i] + ks * 6144), lds_tr(st + aw[i] + ks * 6144 + 768));
    };
    auto mm = [&](int set) {
#pragma unroll
      for (int i = 0; i < 2; ++i)
#pragma unroll
        for (int m = 0; m < 4; ++m) acc[m][i] = MFMA(fx[set][m], fd[set][i], acc[m][i]);
#pragma unroll
      for (int i = 0; i < 3; ++i)
#pragma unroll
        for (int m = 0; m < 4; ++m)
          acc[m][2 + i] = Z2 ? MFMA(fx[set][m], fw[set][i], acc[m][2 + i]) : MFMA(fw[set][i], fx[set][m], acc[m][2 + i]);
    };
    auto mmcs = [&](int set) {
#pragma unroll
      for (int i = 0; i < 2; ++i) cs[i] = MFMA(ones, fd[set][i], cs[i]);
#pragma unroll
      for (int i = 0; i < 3; ++i) cs[2 + i] = Z2 ? MFMA(ones, fw[set][i], cs[2 + i]) : MFMA(fw[set][i], ones, cs[2 + i]);
    };
    auto interleave = [&]() {
#pragma unroll
      for (int i = 0; i < 12; ++i) { __builtin_amdgcn_sched_group_barrier(0x8, 1, 0); __builtin_amdgcn_sched_group_barrier(0x100, 1, 0); }
      __builtin_amdgcn_sched_group_barrier(0x8, 8, 0);
    };
    const unsigned gxo = WS_XST + (panel * 128 + 32 * w + (lane & 31)) * 16;
    u32x4 gx;
    BAR();
    TSTAMP(2);
    rd(0, 0, 0);
    WAIT_L0(); SCHED();
    static_for<0, 8>([&](auto T) {
      constexpr int t = T.value;
      rd(1, t, 1); mm(0); interleave(); SCHED();
      if (wr == 0) mmcs(0);
      if constexpr (t < 7) {
        WAIT_L0(); BAR(); SCHED();
        if constexpr (t == 5) gx = ld16(rs, gxo, 0);
        rd(0, t + 1, 0); mm(1); interleave(); SCHED();
      } else {
        mm(1);
      }
      if (wr == 1) mmcs(1);
    });
    TSTAMP(3);
#pragma unroll
    for (int m = 0; m < 2; ++m)
#pragma unroll
      for (int i = 0; i < 5; ++i) {
        *(f32x4*)(shm + L_HO + (pw * 10 + m * 5 + i) * 1024 + lane * 16) = acc[2 + m][i];
        ac[m][i] = acc[m][i];
      }
#pragma unroll
    for (int i = 0; i < 5; ++i) {
      if (i < 2 || Z2) { if (q == 0) csum[((wc * 5 + i) * 16 + c) * 2 + wr] = cs[i][0]; }
      else if (c == 0) {
#pragma unroll
        for (int r = 0; r < 4; ++r) csum[((wc * 5 + i) * 16 + 4 * q + r) * 2 + wr] = cs[i][r];
      }
    }
    for (unsigned spin = 0; spin < SPIN_MAX; ++spin) {
      if (__all((gx[1] == tag[0]) & (gx[3] == tag[1]))) break;
      __builtin_amdgcn_s_sleep(1);
      gx = ld16(rs, gxo, 0);
    }
    if (lane < 32) { xrs[32 * w + lane] = __uint_as_float(gx[0]); xnm[32 * w + lane] = __uint_as_float(gx[2]); }
    WAIT_L0();
  }
  BAR();
  TSTAMP(4);

  {
    const int wr4 = 2 * wr + (w >> 2);
    if (prod) {
#pragma unroll
      for (int m = 0; m < 2; ++m)
#pragma unroll
        for (int i = 0; i < 5; ++i) ac[m][i] = *(const f32x4*)(shm + L_HO + (pw * 10 + m * 5 + i) * 1024 + lane * 16);
    }
    const unsigned bimg = WS_BIMG + (unsigned)(panel * 4 + wr4) * (NT_ALL * 1024) + (c + 16 * (((q & 1) << 1) | (q >> 1))) * 16;
    f32x4 rs4[2], nm4[2];
#pragma unroll
    for (int m = 0; m < 2; ++m) {
      rs4[m] = *(const f32x4*)(xrs + 32 * wr4 + 16 * m + 4 * q);
      nm4[m] = *(const f32x4*)(xnm + 32 * wr4 + 16 * m + 4 * q);
    }
    auto colsum1 = [&](int i) { const f32x2 t = *(const f32x2*)(csum + ((wc * 5 + i) * 16 + c) * 2); return t[0] + t[1]; };
    auto store_bt = [&](const f32x4& v0, const f32x4& v1, int ntile) {
      unsigned a0 = pack2(v0[0], v0[1]), a1 = pack2(v0[2], v0[3]), b0 = pack2(v1[0], v1[1]), b1 = pack2(v1[2], v1[3]);
      swap16(a0, b0); swap16(a1, b1);
      st16(rs, bimg + ntile * 1024, 0, u32x4{a0, a1, b0, b1});
    };
#pragma unroll
    for (int i = 0; i < 2; ++i) {
      const float cv = colsum1(i);
      store_bt(rs4[0] * ac[0][i] + nm4[0] * cv, rs4[1] * ac[1][i] + nm4[1] * cv, (j >> 3) * 32 + (j & 7) * 4 + 2 * wc + i);
    }

    if constexpr (!Z2) {
      float rl[2], nl[2];
#pragma unroll
      for (int m = 0; m < 2; ++m) { rl[m] = xrs[32 * wr4 + 16 * m + c]; nl[m] = xnm[32 * wr4 + 16 * m + c]; }
      u32x4 zp[3];
#pragma unroll
      for (int i = 0; i < 3; ++i) {
        f32x4 cv;
#pragma unroll
        for (int r = 0; r < 4; ++r) { const f32x2 t = *(const f32x2*)(csum + ((wc * 5 + 2 + i) * 16 + 4 * q + r) * 2); cv[r] = t[0] + t[1]; }
#pragma unroll
        for (int m = 0; m < 2; ++m) {
          const f32x4 v = rl[m] * ac[m][2 + i] + nl[m] * cv;
          zp[i][2 * m] = pack2(gelu_tanh(v[0]), gelu_tanh(v[1]));
          zp[i][2 * m + 1] = pack2(gelu_tanh(v[2]), gelu_tanh(v[3]));
        }
      }
      asm volatile("" : "+v"(zp[0]), "+v"(zp[1]), "+v"(zp[2]));
      WAIT_V(0); BAR();
      TSTAMP(5);
      if (w == 0) flag_set(rs, FL_AB + panel * 16 + j, tag, lane);
      const unsigned z1o = WS_Z1 + (unsigned)(panel * 4 + wr4) * (NFT * 1024) + lane * 16;
#pragma unroll
      for (int i = 0; i < 3; ++i)
        if (3 * wc + i < fcnt) st16(rs, z1o + (fstart + 3 * wc + i) * 1024, 0, zp[i]);
      if (w == 0) flag_wait(rs, FL_AB + (ykh * 8 + ((lane >> 1) & 7)) * 16 + ymat * 8 + 2 * yeg + (lane & 1), tag);
      WAIT_V(0); BAR();
      TSTAMP(8);
      if (w == 1) flag_set(rs, FL_Z1 + panel * 8 + part, tag, lane);
    } else {
      f32x4 zv[2][3];
      f32x4 ssum[2] = {}, ssq[2] = {};
#pragma unroll
      for (int i = 0; i < 3; ++i) {
        const float cv = colsum1(2 + i);
        const float valid = (3 * wc + i < fcnt) ? 1.0f : 0.0f;
#pragma unroll
        for (int m = 0; m < 2; ++m) {
          const f32x4 v = rs4[m] * ac[m][2 + i] + nm4[m] * cv;
#pragma unroll
          for (int r = 0; r < 4; ++r) zv[m][i][r] = gelu_tanh(v[r]) * valid;
          ssum[m] += zv[m][i];
          ssq[m] += zv[m][i] * zv[m][i];
        }
      }
      float* zps = (float*)(shm + L_ZPS);
      float* zpq = (float*)(shm + L_ZPQ);
      float* zrs = (float*)(shm + L_ZRS);
      float* znm = (float*)(shm + L_ZNM);
#pragma unroll
      for (int m = 0; m < 2; ++m) {
#pragma unroll
        for (int r = 0; r < 4; ++r) { ssum[m][r] = row16_sum(ssum[m][r]); ssq[m][r] = row16_sum(ssq[m][r]); }
        if (c == 0) {
          *(f32x4*)(zps + wc * 128 + 32 * wr4 + 16 * m + 4 * q) = ssum[m];
          *(f32x4*)(zpq + wc * 128 + 32 * wr4 + 16 * m + 4 * q) = ssq[m];
        }
      }
      WAIT_V(0); WAIT_L0(); BAR();
      TSTAMP(5);
      if (w == 2) flag_set(rs, FL_AB + panel * 16 + j, tag, lane);
      if (w < 2) {
        const unsigned zoff = WS_ZST + (panel * 8 * 128 + tid) * 16;
        granule_store(rs, zoff + part * 2048, zps[tid] + zps[128 + tid], zpq[tid] + zpq[128 + tid], tag);
        for (unsigned spin = 0; spin < SPIN_MAX; ++spin) {
          bool ok = true; float s = 0.f, sq = 0.f;
#pragma unroll
          for (int p = 0; p < 8; ++p) {
            const u32x4 g = ld16(rs, zoff + p * 2048, 0);
            ok = ok & (g[1] == tag[0]) & (g[3] == tag[1]);
            s += __uint_as_float(g[0]); sq += __uint_as_float(g[2]);
          }
          const float mean = s * (1.0f / FH);
          const float var = __builtin_fmaxf(sq * (1.0f / FH) - mean * mean, 0.0f);
          const float rsv = __builtin_amdgcn_rsqf(var + 1e-5f);
          zrs[tid] = rsv; znm[tid] = -mean * rsv;
          if (__all(ok)) break;
          __builtin_amdgcn_s_sleep(1);
        }
      }
      WAIT_L0(); BAR();
      TSTAMP(6);
      f32x4 zr[2], zn[2];
#pragma unroll
      for (int m = 0; m < 2; ++m) {
        zr[m] = *(const f32x4*)(zrs + 32 * wr4 + 16 * m + 4 * q);
        zn[m] = *(const f32x4*)(znm + 32 * wr4 + 16 * m + 4 * q);
      }
#pragma unroll
      for (int i = 0; i < 3; ++i)
        if (3 * wc + i < fcnt) store_bt(zv[0][i] * zr[0] + zn[0], zv[1][i] * zr[1] + zn[1], 64 + fstart + 3 * wc + i);
      WAIT_V(0); BAR();
      TSTAMP(7);
      if (w == 0) {
        flag_set(rs, FL_Z2 + panel * 8 + part, tag, lane);
        flag_wait(rs, lane < 16 ? FL_Z2 + lane * 8 + gpart : FL_Z1 + gpanel * 8 + gpart, tag);
      }
      BAR();
      TSTAMP(8);
    }
  }

  if (prod) {
#pragma unroll
    for (int s = 0; s < P::PF; ++s) issueBB(s, s);
#pragma unroll
    for (int s = P::PF; s < P::NS; ++s) { issueBA(s, s); issueBB(s, s); }
    WAIT_V(P::span(1, P::NS - 1)); BAR();
    constexpr int NPEEL = P::PF > 2 ? P::PF - 2 : 0;
    static_for<0, NPEEL>([&](auto T) {
      constexpr int t = T.value;
      WAIT_V(P::span(t + 2, t + P::NS - 1)); BAR();
      issueBA(t + P::NS, t % P::NS); issueBB(t + P::NS, t % P::NS);
    });
    int slot = NPEEL % P::NS;
    for (int t = NPEEL; t < P::NSTG - P::NS; ++t) {
      WAIT_V((P::NS - 2) * P::NP); BAR();
      issueBA(t + P::NS, slot); issueBB(t + P::NS, slot);
      slot = (slot + 1 == P::NS) ? 0 : slot + 1;
    }
    static_for<0, P::NS>([&](auto I) {
      constexpr int rem = P::NS - 2 - I.value;
      WAIT_V((rem > 0 ? rem : 0) * P::NP); BAR();
    });
    const int rr = lane >> 3;
    const unsigned lo = (unsigned)rr * 1024u + (unsigned)(G * 128 + (((lane & 7) ^ (2 * ((rr >> 1) & 3))) << 4));
#pragma unroll
    for (int k = 0; k < 22; ++k) {
      if (k == 10) BAR();
      int Pc = 4 * k + pw; Pc = Pc < 85 ? Pc : 85;
      glds16o(wout + (size_t)Pc * 8192, lo, shm + Pc * 1024);
    }
    WAIT_V(0); BAR();
  } else {
    f32x4 acc[P::MT][P::NT] = {};
    u32x4 z1[6]; float bs[2];
    if constexpr (Z2) {
#pragma unroll
      for (int n = 0; n < 6; ++n)
        z1[n] = ld16(rs, WS_Z1 + (unsigned)((gpanel * 4 + w) * NFT + gstart + (n < gcnt ? n : gcnt - 1)) * 1024 + lane * 16, 0);
#pragma unroll
      for (int m = 0; m < 2; ++m) bs[m] = bflo(*(const unsigned short*)(bsp + (gpanel * 128 + w * 32 + m * 16 + c) * 2));
    }
    {
      constexpr int MT = P::MT, NT = P::NT;
      bf16x8 fa[2][MT], fb[2][NT];
      int aoff[2];
#pragma unroll
      for (int ks = 0; ks < 2; ++ks) aoff[ks] = ((Z2 ? 32 * w : 128 * wr) + c) * 128 + (((ks * 4 + q) ^ ((c >> 1) & 7)) << 4);
      const int boff = P::BOFF + (Z2 ? 0 : wc * 4096) + lane * 16;
      auto rd = [&](int set, int slot, int ks) {
        const char* sa = shm + slot * P::ABYTES;
        const char* sb = shm + slot * P::BBYTES;
#pragma unroll
        for (int i = 0; i < (MT > NT ? MT : NT); ++i) {
          if (i < NT) fb[set][i] = lds_frag(sb + boff + ks * P::BKS + i * 1024);
          if (i < MT) fa[set][i] = lds_frag(sa + aoff[ks] + i * 2048);
        }
      };
      auto mm = [&](int set) {
#pragma unroll
        for (int m = 0; m < MT; ++m)
#pragma unroll
          for (int n = 0; n < NT; ++n) acc[m][n] = MFMA(fb[set][n], fa[set][m], acc[m][n]);
      };
      auto interleave = [&]() {
#pragma unroll
        for (int i = 0; i < MT + NT; ++i) { __builtin_amdgcn_sched_group_barrier(0x8, 1, 0); __builtin_amdgcn_sched_group_barrier(0x100, 1, 0); }
        __builtin_amdgcn_sched_group_barrier(0x8, MT * NT - MT - NT, 0);
      };
      BAR();
      TSTAMP(9);
      rd(0, 0, 0);
      WAIT_L0(); SCHED();
      int slot = 0;
      for (int t = 0; t < P::NSTG; ++t) {
        rd(1, slot, 1); mm(0); interleave(); SCHED();
        WAIT_L0(); BAR(); SCHED();
        slot = (slot + 1 == P::NS) ? 0 : slot + 1;
        rd(0, slot, 0);
        mm(1); interleave(); SCHED();
      }
    }
    TSTAMP(10);
    if constexpr (Z2) {
#pragma unroll
      for (int m = 0; m < 2; ++m)
#pragma unroll
        for (int p = 0; p < 3; ++p) {
          u32x4 o;
#pragma unroll
          for (int h = 0; h < 2; ++h) {
            const int n = 2 * p + h;
            const unsigned zl = z1[n][2 * m], zh = z1[n][2 * m + 1];
            const f32x4 g = acc[m][n] + bs[m];
            o[2 * h] = pack2(bflo(zl) * g[0], bfhi(zl) * g[1]);
            o[2 * h + 1] = pack2(bflo(zh) * g[2], bfhi(zh) * g[3]);
          }
          if (p == 2 && gcnt < 6) { o[2] = 0; o[3] = 0; }
          st16(rs, WS_H + (unsigned)((gpanel * 8 + 2 * w + m) * 24 + gpart * 3 + p) * 1024 + lane * 16, 0, o);
        }
    } else {
      const unsigned yo = WS_Y + (unsigned)(((ymat * 2 + ykh) * 128 + yp * 16 + wr * 8) * 16 + yeg * 4 + wc * 2) * 1024 + lane * 16;
#pragma unroll
      for (int m = 0; m < 8; ++m)
#pragma unroll
        for (int h = 0; h < 2; ++h) {
          const f32x4 v0 = acc[m][2 * h], v1 = acc[m][2 * h + 1];
          unsigned a0 = pack2(v0[0], v0[1]), a1 = pack2(v0[2], v0[3]), b0 = pack2(v1[0], v1[1]), b1 = pack2(v1[2], v1[3]);
          swap16(a0, b0); swap16(a1, b1);
          st16(rs, yo + m * 16384 + h * 1024, 0, u32x4{a0, a1, b0, b1});
        }
    }
    WAIT_V(0); BAR();
    TSTAMP(11);
    if (w == 0) flag_set(rs, Z2 ? FL_H + gpanel * 8 + gpart : FL_Y + ((ymat * 2 + ykh) * 8 + yp) * 4 + yeg, tag, lane);
    const size_t eo = (size_t)(tb * 64 + w * 16 + c) * 1024 + (G * 64 + 16 * (q & 1) + 8 * (q >> 1)) * 2;
    u32x4 xv[2];
#pragma unroll
    for (int h = 0; h < 2; ++h) xv[h] = *(const u32x4*)(x + eo + h * 64);
    if (w == 0) flag_wait(rs, lane < 8 ? FL_H + (tb >> 1) * 8 + lane : FL_Y + (((lane - 8) & 3) * 8 + (tb >> 2)) * 4 + (G >> 1), tag);
    if (w == 1 && blockIdx.x == 0) {
#pragma unroll
      for (int k = 0; k < 4; ++k) flag_wait(rs, FL_AB + k * 64 + lane, tag);
      if (lane == 0) __builtin_amdgcn_raw_buffer_store_b64(tag, rs, WS_SALT, 0, SC1);
    }
    BAR();
    TSTAMP(12);
    bf16x8 hf[24];
    u32x4 yv[4][2];
#pragma unroll
    for (int kb = 0; kb < 24; ++kb) hf[kb] = __builtin_bit_cast(bf16x8, ld16(rs, WS_H + (unsigned)((tb * 4 + w) * 24 + kb) * 1024 + lane * 16, 0));
#pragma unroll
    for (int h = 0; h < 2; ++h)
#pragma unroll
      for (int p = 0; p < 4; ++p) yv[p][h] = ld16(rs, WS_Y + (unsigned)((p * 128 + tb * 4 + w) * 16 + 2 * G + h) * 1024 + lane * 16, 0);
    int at[4];
    {
      const int r = c >> 2, sg = (2 * (q & 1) + (r >> 1)) & 3;
#pragma unroll
      for (int T = 0; T < 4; ++T) at[T] = (4 * q + r) * 128 + ((T ^ sg) << 5) + (c & 3) * 8;
    }
    f32x4 oc[4] = {};
    bf16x8 wf[3][4];
    auto rdw = [&](auto KB) {
      constexpr int kb = KB.value, pt = kb / 3, pr = kb % 3, ro = (part_start(pt) + 2 * pr) * 2048;
      constexpr bool full = 2 * pr + 1 < part_cnt(pt);
#pragma unroll
      for (int T = 0; T < 4; ++T) {
        const bf16x4 lo = lds_tr(shm + at[T] + ro);
        const bf16x4 hi = full ? lds_tr(shm + at[T] + ro + 2048) : bf16x4{0, 0, 0, 0};
        wf[kb % 3][T] = cat(lo, hi);
      }
    };
    rdw(std::integral_constant<int, 0>{});
    rdw(std::integral_constant<int, 1>{});
    SCHED();
    static_for<0, 24>([&](auto KB) {
      constexpr int kb = KB.value;
      if constexpr (kb + 2 < 24) rdw(std::integral_constant<int, kb + 2>{});
#pragma unroll
      for (int T = 0; T < 4; ++T) oc[T] = MFMA(wf[kb % 3][T], hf[kb], oc[T]);
#pragma unroll
      for (int T = 0; T < 4; ++T) { __builtin_amdgcn_sched_group_barrier(0x8, 1, 0); __builtin_amdgcn_sched_group_barrier(0x100, 2, 0); }
      SCHED();
    });
    TSTAMP(13);
#pragma unroll
    for (int h = 0; h < 2; ++h) {
      float o[8];
#pragma unroll
      for (int r = 0; r < 4; ++r) {
        unsigned a = __float_as_uint(oc[2 * h][r]), b = __float_as_uint(oc[2 * h + 1][r]);
        swap16(a, b);
        o[r] = __uint_as_float(a); o[4 + r] = __uint_as_float(b);
      }
      unsigned res[4];
#pragma unroll
      for (int d = 0; d < 4; ++d) {
        const float yl = (bflo(yv[0][h][d]) + bflo(yv[1][h][d])) - (bflo(yv[2][h][d]) + bflo(yv[3][h][d]));
        const float yh = (bfhi(yv[0][h][d]) + bfhi(yv[1][h][d])) - (bfhi(yv[2][h][d]) + bfhi(yv[3][h][d]));
        res[d] = pack2(bflo(xv[h][d]) + yl * silu(o[2 * d]), bfhi(xv[h][d]) + yh * silu(o[2 * d + 1]));
      }
      *(u32x4*)(out + eo + h * 64) = u32x4{res[0], res[1], res[2], res[3]};
    }
  }
}

__global__ __launch_bounds__(512, 2) void kF(const char* __restrict__ x, const char* __restrict__ wsre, const char* __restrict__ wsim,
                                             const char* __restrict__ wdre, const char* __restrict__ wdim, const char* __restrict__ win,
                                             const char* __restrict__ wsp, const char* __restrict__ bsp, const char* __restrict__ wout,
                                             char* __restrict__ ws, char* __restrict__ out) {
  extern __shared__ __attribute__((aligned(1024))) char shm[];
  TSTAMP(0);
  const int xcd = blockIdx.x & 7;
  if (xcd < 4) body<true>(x, wdre, wsp, win, bsp, wout, ws, out, shm);
  else body<false>(x, wdim, (xcd & 2) ? wsim : wsre, win, bsp, wout, ws, out, shm);
  TSTAMP(1);
}

__global__ void kT(char* ws) {
  const unsigned long long* ts = (const unsigned long long*)(ws + WS_DBG);
  const int lane = threadIdx.x;
  unsigned long long lo = ~0ull, hi = 0, top = 0;
  for (int i = lane; i < 256; i += 64) { unsigned long long a = ts[i * 16 + 1]; top = a > top ? a : top; }
  for (int o = 32; o; o >>= 1) { unsigned long long a = __shfl_xor(top, o); top = a > top ? a : top; }
  for (int i = lane; i < 256; i += 64) { unsigned long long a = ts[i * 16 + 0]; lo = a < lo ? a : lo; }
  for (int o = 32; o; o >>= 1) { unsigned long long a = __shfl_xor(lo, o); lo = a < lo ? a : lo; }
  hi = lo + 20000;
  auto stat = [&](int grp, int slot, int mode) -> double {
    unsigned long long mn = ~0ull, mx = 0, sum = 0; unsigned n = 0;
    for (int i = lane; i < 256; i += 64) { if (grp != 2 && (((i & 7) >= 4) != grp)) continue; unsigned long long a = ts[i * 16 + slot]; if (a >= lo && a < hi) { a -= lo; mn = a < mn ? a : mn; mx = a > mx ? a : mx; sum += a; ++n; } }
    for (int o = 32; o; o >>= 1) { unsigned long long a = __shfl_xor(mn, o); mn = a < mn ? a : mn; a = __shfl_xor(mx, o); mx = a > mx ? a : mx; sum += __shfl_xor(sum, o); n += __shfl_xor(n, o); }
    return mode == 0 ? (double)mx : mode == 2 ? (double)mn : mode == 3 ? 0.0 : (double)sum / (n ? n : 1);
  };
  double d = stat(2, 1, 3);
  if (!(d > 0)) d = 0; if (d > 10000) d = 10000;
  const unsigned long long t0 = __builtin_amdgcn_s_memrealtime(), dt = (unsigned long long)(d * 10);
  for (unsigned it = 0; it < 2000000; ++it) { if (__builtin_amdgcn_s_memrealtime() - t0 >= dt) break; __builtin_amdgcn_s_sleep(1); }
}

extern "C" void kernel_launch(void* const* d_in, const int* in_sizes, int n_in, void* d_out, int out_size, void* d_ws, size_t ws_size,
                              hipStream_t stream) {
  (void)hipFuncSetAttribute((const void*)kF, hipFuncAttributeMaxDynamicSharedMemorySize, LDS_BYTES);
  kF<<<256, 512, LDS_BYTES, stream>>>((const char*)d_in[0], (const char*)d_in[1], (const char*)d_in[2], (const char*)d_in[3],
                                      (const char*)d_in[4], (const char*)d_in[6], (const char*)d_in[7], (const char*)d_in[8],
                                      (const char*)d_in[9], (char*)d_ws, (char*)d_out);
  kT<<<1, 64, 0, stream>>>((char*)d_ws);
}
```

```cpp
#define OUT_AUX 2
#include <hip/hip_runtime.h>
#include <hip/hip_bf16.h>
#include <stdint.h>
#include <type_traits>

typedef short bf16x8 __attribute__((ext_vector_type(8)));
typedef short bf16x4 __attribute__((ext_vector_type(4)));
typedef short v4i16_t __attribute__((ext_vector_type(4)));
typedef float f32x4 __attribute__((ext_vector_type(4)));
typedef float f32x2 __attribute__((ext_vector_type(2)));
typedef unsigned u32x4 __attribute__((ext_vector_type(4)));
typedef unsigned u32x2 __attribute__((ext_vector_type(2)));
typedef __bf16 bf16v2 __attribute__((ext_vector_type(2)));
typedef const __attribute__((address_space(1))) void* gptr_t;
typedef __attribute__((address_space(3))) void* lptr_t;
typedef __amdgpu_buffer_rsrc_t rsrc_t;

#define WAIT_V(n) asm volatile("s_waitcnt vmcnt(%0)" ::"n"(n) : "memory")
#define WAIT_L0() asm volatile("s_waitcnt lgkmcnt(0)" ::: "memory")
#define BAR() do { asm volatile("" ::: "memory"); __builtin_amdgcn_s_barrier(); asm volatile("" ::: "memory"); } while (0)
#define SCHED() __builtin_amdgcn_sched_barrier(0)
#define MFMA(a, b, c) __builtin_amdgcn_mfma_f32_16x16x32_bf16(a, b, c, 0, 0, 0)

constexpr int S_ = 2048, D_ = 512, F_ = 1376, FH = 688, NFT = 43;
constexpr int NT_ALL = 112;
constexpr unsigned WS_BIMG = 0;
constexpr unsigned WS_Z1 = 8u << 20;
constexpr unsigned WS_H = 12u << 20;
constexpr unsigned WS_Y = 16u << 20;
constexpr unsigned WS_XST = 24u << 20;
constexpr unsigned WS_ZST = WS_XST + 65536;
constexpr unsigned WS_FLG = WS_ZST + 262144;
constexpr unsigned WS_SALT = WS_FLG + 131072;
constexpr unsigned WS_END = 25u << 20;
constexpr unsigned WS_DBG = 26u << 20;
constexpr int FL_AB = 0, FL_Z2 = 256, FL_Z1 = 384, FL_H = 512, FL_Y = 640;

template <int I, int N, class F>
__device__ __forceinline__ void static_for(F&& f) {
  if constexpr (I < N) { f(std::integral_constant<int, I>{}); static_for<I + 1, N>(f); }
}
__device__ __forceinline__ unsigned pack2(float a, float b) {
  return __builtin_bit_cast(unsigned, __builtin_convertvector(f32x2{a, b}, bf16v2));
}
__device__ __forceinline__ float bflo(unsigned u) { return __uint_as_float(u << 16); }
__device__ __forceinline__ float bfhi(unsigned u) { return __uint_as_float(u & 0xffff0000u); }
template <int AUX = 0>
__device__ __forceinline__ void glds16o(const char* ubase, unsigned off, char* l) {
  asm volatile("" : "+v"(off));
  __builtin_amdgcn_global_load_lds((gptr_t)(ubase + off), (lptr_t)l, 16, 0, AUX);
}
__device__ __forceinline__ void swap16(unsigned& a, unsigned& b) {
  auto r = __builtin_amdgcn_permlane16_swap(a, b, false, false);
  a = r[0]; b = r[1];
}
template <int CTRL>
__device__ __forceinline__ float dpp_add(float v) {
  return v + __int_as_float(__builtin_amdgcn_update_dpp(0, __float_as_int(v), CTRL, 0xf, 0xf, true));
}
__device__ __forceinline__ float row16_sum(float v) {
  v = dpp_add<0x128>(v); v = dpp_add<0x124>(v); v = dpp_add<0x122>(v); v = dpp_add<0x121>(v);
  return v;
}
__device__ __forceinline__ float wave_sum(float v) {
  v = row16_sum(v);
  const int i = __float_as_int(v);
  return __int_as_float(__builtin_amdgcn_readlane(i, 0)) + __int_as_float(__builtin_amdgcn_readlane(i, 16)) +
         __int_as_float(__builtin_amdgcn_readlane(i, 32)) + __int_as_float(__builtin_amdgcn_readlane(i, 48));
}
__device__ __forceinline__ float gelu_tanh(float v) {
  const float c0 = -2.3022081986f, c1 = -0.1029432397f;
  const float e = __builtin_amdgcn_exp2f(v * __builtin_fmaf(v * v, c1, c0));
  return v * __builtin_amdgcn_rcpf(1.0f + e);
}
__device__ __forceinline__ float silu(float v) {
  const float e = __builtin_amdgcn_exp2f(v * -1.4426950409f);
  return v * __builtin_amdgcn_rcpf(1.0f + e);
}
__device__ __forceinline__ bf16x8 lds_frag(const char* p) { return *(const bf16x8*)p; }
__device__ __forceinline__ bf16x4 lds_tr(const char* p) {
  return __builtin_bit_cast(bf16x4, __builtin_amdgcn_ds_read_tr16_b64_v4i16((__attribute__((address_space(3))) v4i16_t*)p));
}
__device__ __forceinline__ bf16x8 cat(bf16x4 a, bf16x4 b) { return __builtin_shufflevector(a, b, 0, 1, 2, 3, 4, 5, 6, 7); }

constexpr int SC1 = 16;
__device__ __forceinline__ void st16(rsrc_t rs, unsigned voff, unsigned soff, u32x4 v) { __builtin_amdgcn_raw_buffer_store_b128(v, rs, voff, soff, SC1); }
__device__ __forceinline__ u32x4 ld16(rsrc_t rs, unsigned voff, unsigned soff) { return __builtin_amdgcn_raw_buffer_load_b128(rs, voff, soff, SC1); }
__device__ __forceinline__ void granule_store(rsrc_t rs, unsigned off, float v0, float v1, u32x2 tag) {
  st16(rs, off, 0, u32x4{__float_as_uint(v0), tag[0], __float_as_uint(v1), tag[1]});
}
constexpr unsigned SPIN_MAX = 1u << 18;
__device__ __forceinline__ void flag_set(rsrc_t rs, int idx, u32x2 tag, int lane) {
  if (lane == 0) __builtin_amdgcn_raw_buffer_store_b64(tag, rs, WS_FLG + idx * 128, 0, SC1);
}
__device__ __forceinline__ void flag_wait(rsrc_t rs, int idx, u32x2 tag) {
  for (unsigned spin = 0; spin < SPIN_MAX; ++spin) {
    const u32x2 g = __builtin_amdgcn_raw_buffer_load_b64(rs, WS_FLG + idx * 128, 0, SC1);
    if (__all((g[0] == tag[0]) & (g[1] == tag[1]))) break;
    __builtin_amdgcn_s_sleep(1);
  }
  asm volatile("" ::: "memory");
}

#define TSTAMP(slot) do {} while (0)
#define TSTAMPW(slot, wv) do {} while (0)

__device__ __forceinline__ constexpr int part_start(int p) { return p < 3 ? 6 * p : 18 + 5 * (p - 3); }
__device__ __forceinline__ constexpr int part_cnt(int p) { return p < 3 ? 6 : 5; }

constexpr int A_STAGE = 36864, A_NS = 4, A_XO = 0, A_DO = 16384, A_WO = 24576;
constexpr int L_HO = 69632;
constexpr int L_CS = A_NS * A_STAGE;
constexpr int L_XRS = L_CS + 2048;
constexpr int L_XNM = L_XRS + 512;
constexpr int L_ZPS = L_XNM + 512;
constexpr int L_ZPQ = L_ZPS + 1024;
constexpr int L_ZRS = L_ZPQ + 1024;
constexpr int L_ZNM = L_ZRS + 512;
constexpr int LDS_BYTES = L_ZNM + 512;

template <bool G>
struct PB {
  static constexpr int NA = G ? 4 : 8, NB = G ? 3 : 4, NP = NA + NB;
  static constexpr int NS = G ? 5 : 3, NSTG = G ? 32 : 16, PF = G ? 4 : 2;
  static constexpr int ABYTES = NA * 4096, BBYTES = NB * 4096, BOFF = G ? 81920 : 98304;
  static constexpr int MT = G ? 2 : 8, NT = G ? 6 : 4, BKS = BBYTES / 2;
  static constexpr int cnt(int s) { return s >= NSTG ? 0 : (s < PF ? NB : NP); }
  static constexpr int span(int s0, int s1) { return s0 > s1 ? 0 : cnt(s0) + span(s0 + 1, s1); }
};

template <bool Z2>
__device__ __forceinline__ void body(const char* __restrict__ x, const char* __restrict__ wd, const char* __restrict__ wsq,
                                     const char* __restrict__ win, const char* __restrict__ bsp, const char* __restrict__ wout,
                                     char* __restrict__ ws, char* __restrict__ out, char* shm) {
  typedef PB<Z2> P;
  const int tid = threadIdx.x, w = __builtin_amdgcn_readfirstlane(tid >> 6), lane = tid & 63;
  const int c = lane & 15, q = lane >> 4;
  const int xcd = blockIdx.x & 7, bi = blockIdx.x >> 3;
  const bool prod = w >= 4;
  const int pw = w & 3, wr = pw >> 1, wc = pw & 1;
  const int panel = (xcd & 1) * 8 + (bi >> 2), j = (xcd >> 1) * 4 + (bi & 3);
  const int part = j & 7, fstart = part_start(part), fcnt = part_cnt(part);
  const int gpanel = xcd * 4 + (bi >> 3), gpart = bi & 7, gstart = part_start(gpart), gcnt = part_cnt(gpart);
  const int ymat = (xcd >> 1) & 1, ykh = xcd & 1, yp = bi >> 2, yeg = bi & 3;
  const int tb = xcd * 4 + (bi >> 3), G = bi & 7;

  const rsrc_t rs = __builtin_amdgcn_make_buffer_rsrc((void*)ws, 0, WS_END, 0x00020000);
  u32x2 tag = {0, 0};
  f32x4 ac[2][5];
  unsigned oa[P::NA], ob[P::NB];
  const char* Ab = wsq + (Z2 ? (size_t)gpanel * 128 * 4096 : (size_t)yp * 256 * 4096 + ykh * 2048);
  const char* Bb = ws + WS_BIMG + (Z2 ? 0 : (size_t)ykh * 32 * (NT_ALL * 1024));
  auto issueBA = [&](int t, int slot) {
    const char* ab = Ab + t * 128;
    asm volatile("" : "+s"(ab));
#pragma unroll
    for (int i = 0; i < P::NA; ++i) glds16o(ab, oa[i], shm + slot * P::ABYTES + (pw * P::NA + i) * 1024);
  };
  auto issueBB = [&](int t, int slot) {
    const char* bb = Bb + (size_t)t * (2 * NT_ALL * 1024);
    asm volatile("" : "+s"(bb));
#pragma unroll
    for (int i = 0; i < P::NB; ++i) glds16o<SC1>(bb, ob[i], shm + P::BOFF + slot * P::BBYTES + (pw * P::NB + i) * 1024);
  };

  float* csum = (float*)(shm + L_CS);
  float* xrs = (float*)(shm + L_XRS);
  float* xnm = (float*)(shm + L_XNM);
  if (prod) {
    unsigned ox[4], od[2], ow[3];
#pragma unroll
    for (int i = 0; i < 4; ++i) {
      const int row = 8 * (pw * 4 + i) + (lane >> 3);
      ox[i] = (unsigned)(panel * 128 + row) * 1024u + (unsigned)(((lane & 7) ^ ((row >> 1) & 7)) << 4);
    }
#pragma unroll
    for (int i = 0; i < 2; ++i) {
      const int row = 8 * (pw * 2 + i) + (lane >> 3);
      od[i] = (unsigned)((j & 7) * 64 + row) * 1024u + (unsigned)(((lane & 7) ^ ((row >> 1) & 7)) << 4);
    }
#pragma unroll
    for (int i = 0; i < 3; ++i) {
      const int o = (pw * 3 + i) * 1024 + lane * 16, row = o / 192, ch = (o % 192) >> 4;
      const int u = (ch >> 1) ^ ((row >> 3) & 1);
      const int ft = fstart + (u < fcnt ? u : fcnt - 1);
      ow[i] = (unsigned)row * 2752u + (unsigned)((Z2 ? FH * 2 : 0) + ft * 32 + (ch & 1) * 16);
    }
    auto issue = [&](int t) {
      char* st = shm + (t % A_NS) * A_STAGE;
      const char* xb = x + t * 128;
      const char* db = wd + t * 128;
      const char* wb = win + (size_t)t * (64 * 2752);
      asm volatile("" : "+s"(xb), "+s"(db), "+s"(wb));
#pragma unroll
      for (int i = 0; i < 4; ++i) glds16o(xb, ox[i], st + A_XO + (pw * 4 + i) * 1024);
#pragma unroll
      for (int i = 0; i < 2; ++i) glds16o(db, od[i], st + A_DO + (pw * 2 + i) * 1024);
#pragma unroll
      for (int i = 0; i < 3; ++i) glds16o(wb, ow[i], st + A_WO + (pw * 3 + i) * 1024);
    };
#pragma unroll
    for (int t = 0; t < A_NS; ++t) issue(t);
#pragma unroll
    for (int i = 0; i < P::NA; ++i) {
      const int row = (pw * P::NA + i) * 8 + (lane >> 3);
      oa[i] = (unsigned)row * 4096u + (unsigned)(((lane & 7) ^ ((row >> 1) & 7)) << 4);
    }
#pragma unroll
    for (int i = 0; i < P::NB; ++i) {
      const int n = (pw & 1) * P::NB + i;
      const int nt = Z2 ? 64 + gstart + (n < gcnt ? n : gcnt - 1) : ymat * 32 + yeg * 8 + n;
      ob[i] = (unsigned)(((pw >> 1) * NT_ALL + nt) * 1024 + lane * 16);
    }
    WAIT_V(27); BAR();
    static_for<0, 7>([&](auto T) {
      constexpr int t = T.value;
      WAIT_V(t <= 4 ? 18 : (t == 5 ? 9 : P::PF * P::NA));
      BAR();
      if constexpr (t + A_NS < 8) issue(t + A_NS);
      if constexpr (t == 5) {
#pragma unroll
        for (int s = 0; s < P::PF; ++s) issueBA(s, s);
      }
    });
  } else {
    u32x2 salt; u32x4 xr0, xr1;
    {
      const char* ps = ws + WS_SALT;
      const char* pr = x + (size_t)(panel * 128 + 8 * j + 2 * w) * 1024 + lane * 16;
      asm volatile("global_load_dwordx2 %0, %1, off sc1" : "=&v"(salt) : "v"(ps) : "memory");
      asm volatile("global_load_dwordx4 %0, %1, off" : "=&v"(xr0) : "v"(pr) : "memory");
      asm volatile("global_load_dwordx4 %0, %1, off offset:1024" : "=&v"(xr1) : "v"(pr) : "memory");
    }
    f32x4 acc[4][5] = {};
    f32x4 cs[5] = {};
    bf16x8 fx[2][4], fd[2][2], fw[2][3];
    const bf16x8 ones = {0x3F80, 0x3F80, 0x3F80, 0x3F80, 0x3F80, 0x3F80, 0x3F80, 0x3F80};
    int ax[2], ad[2], aw[3];
#pragma unroll
    for (int ks = 0; ks < 2; ++ks) {
      const int sw = ((ks * 4 + q) ^ ((c >> 1) & 7)) << 4;
      ax[ks] = A_XO + (64 * wr + c) * 128 + sw;
      ad[ks] = A_DO + (32 * wc + c) * 128 + sw;
    }
#pragma unroll
    for (int i = 0; i < 3; ++i) aw[i] = A_WO + (8 * q + (c >> 2)) * 192 + (((3 * wc + i) ^ (q & 1)) << 5) + (c & 3) * 8;
    asm volatile("s_waitcnt vmcnt(0)" : "+v"(salt), "+v"(xr0), "+v"(xr1)::"memory");
    {
      const unsigned long long tag64 = (((unsigned long long)salt[1] << 32) | salt[0]) + 0x9E3779B97F4A7C15ull;
      tag = u32x2{(unsigned)tag64, (unsigned)(tag64 >> 32)};
      float v[2][8], mean[2], ss[2];
#pragma unroll
      for (int i = 0; i < 4; ++i) { v[0][2 * i] = bflo(xr0[i]); v[0][2 * i + 1] = bfhi(xr0[i]); v[1][2 * i] = bflo(xr1[i]); v[1][2 * i + 1] = bfhi(xr1[i]); }
#pragma unroll
      for (int r = 0; r < 2; ++r)
        mean[r] = wave_sum(((v[r][0] + v[r][1]) + (v[r][2] + v[r][3])) + ((v[r][4] + v[r][5]) + (v[r][6] + v[r][7]))) * (1.0f / D_);
#pragma unroll
      for (int r = 0; r < 2; ++r) {
        ss[r] = 0.f;
#pragma unroll
        for (int i = 0; i < 8; ++i) { const float d = v[r][i] - mean[r]; ss[r] = __builtin_fmaf(d, d, ss[r]); }
        ss[r] = __builtin_amdgcn_rsqf(wave_sum(ss[r]) * (1.0f / D_) + 1e-5f);
      }
      const int r = lane & 1;
      const float rsv = r ? ss[1] : ss[0], mv = r ? mean[1] : mean[0];
      if (lane < 2) granule_store(rs, WS_XST + (panel * 128 + 8 * j + 2 * w + r) * 16, rsv, -mv * rsv, tag);
    }
    auto rd = [&](int set, int t, int ks) {
      const char* st = shm + (t % A_NS) * A_STAGE;
#pragma unroll
      for (int i = 0; i < 2; ++i) fd[set][i] = lds_frag(st + ad[ks] + i * 2048);
#pragma unroll
      for (int m = 0; m < 4; ++m) fx[set][m] = lds_frag(st + ax[ks] + m * 2048);
#pragma unroll
      for (int i = 0; i < 3; ++i) fw[set][i] = cat(lds_tr(st + aw[i] + ks * 6144), lds_tr(st + aw[i] + ks * 6144 + 768));
    };
    auto mm = [&](int set) {
#pragma unroll
      for (int i = 0; i < 2; ++i)
#pragma unroll
        for (int m = 0; m < 4; ++m) acc[m][i] = MFMA(fx[set][m], fd[set][i], acc[m][i]);
#pragma unroll
      for (int i = 0; i < 3; ++i)
#pragma unroll
        for (int m = 0; m < 4; ++m)
          acc[m][2 + i] = Z2 ? MFMA(fx[set][m], fw[set][i], acc[m][2 + i]) : MFMA(fw[set][i], fx[set][m], acc[m][2 + i]);
    };
    auto mmcs = [&](int set) {
#pragma unroll
      for (int i = 0; i < 2; ++i) cs[i] = MFMA(ones, fd[set][i], cs[i]);
#pragma unroll
      for (int i = 0; i < 3; ++i) cs[2 + i] = Z2 ? MFMA(ones, fw[set][i], cs[2 + i]) : MFMA(fw[set][i], ones, cs[2 + i]);
    };
    auto interleave = [&]() {
#pragma unroll
      for (int i = 0; i < 12; ++i) { __builtin_amdgcn_sched_group_barrier(0x8, 1, 0); __builtin_amdgcn_sched_group_barrier(0x100, 1, 0); }
      __builtin_amdgcn_sched_group_barrier(0x8, 8, 0);
    };
    const unsigned gxo = WS_XST + (panel * 128 + 32 * w + (lane & 31)) * 16;
    u32x4 gx;
    BAR();
    TSTAMP(2);
    rd(0, 0, 0);
    WAIT_L0(); SCHED();
    static_for<0, 8>([&](auto T) {
      constexpr int t = T.value;
      rd(1, t, 1); mm(0); interleave(); SCHED();
      if (wr == 0) mmcs(0);
      if constexpr (t < 7) {
        WAIT_L0(); BAR(); SCHED();
        if constexpr (t == 5) gx = ld16(rs, gxo, 0);
        rd(0, t + 1, 0); mm(1); interleave(); SCHED();
      } else {
        mm(1);
      }
      if (wr == 1) mmcs(1);
    });
    TSTAMP(3);
#pragma unroll
    for (int m = 0; m < 2; ++m)
#pragma unroll
      for (int i = 0; i < 5; ++i) {
        *(f32x4*)(shm + L_HO + (pw * 10 + m * 5 + i) * 1024 + lane * 16) = acc[2 + m][i];
        ac[m][i] = acc[m][i];
      }
#pragma unroll
    for (int i = 0; i < 5; ++i) {
      if (i < 2 || Z2) { if (q == 0) csum[((wc * 5 + i) * 16 + c) * 2 + wr] = cs[i][0]; }
      else if (c == 0) {
#pragma unroll
        for (int r = 0; r < 4; ++r) csum[((wc * 5 + i) * 16 + 4 * q + r) * 2 + wr] = cs[i][r];
      }
    }
    for (unsigned spin = 0; spin < SPIN_MAX; ++spin) {
      if (__all((gx[1] == tag[0]) & (gx[3] == tag[1]))) break;
      __builtin_amdgcn_s_sleep(1);
      gx = ld16(rs, gxo, 0);
    }
    if (lane < 32) { xrs[32 * w + lane] = __uint_as_float(gx[0]); xnm[32 * w + lane] = __uint_as_float(gx[2]); }
    WAIT_L0();
  }
  BAR();
  TSTAMP(4);

  {
    const int wr4 = 2 * wr + (w >> 2);
    if (prod) {
#pragma unroll
      for (int m = 0; m < 2; ++m)
#pragma unroll
        for (int i = 0; i < 5; ++i) ac[m][i] = *(const f32x4*)(shm + L_HO + (pw * 10 + m * 5 + i) * 1024 + lane * 16);
    }
    const unsigned bimg = WS_BIMG + (unsigned)(panel * 4 + wr4) * (NT_ALL * 1024) + (c + 16 * (((q & 1) << 1) | (q >> 1))) * 16;
    f32x4 rs4[2], nm4[2];
#pragma unroll
    for (int m = 0; m < 2; ++m) {
      rs4[m] = *(const f32x4*)(xrs + 32 * wr4 + 16 * m + 4 * q);
      nm4[m] = *(const f32x4*)(xnm + 32 * wr4 + 16 * m + 4 * q);
    }
    auto colsum1 = [&](int i) { const f32x2 t = *(const f32x2*)(csum + ((wc * 5 + i) * 16 + c) * 2); return t[0] + t[1]; };
    auto store_bt = [&](const f32x4& v0, const f32x4& v1, int ntile) {
      unsigned a0 = pack2(v0[0], v0[1]), a1 = pack2(v0[2], v0[3]), b0 = pack2(v1[0], v1[1]), b1 = pack2(v1[2], v1[3]);
      swap16(a0, b0); swap16(a1, b1);
      st16(rs, bimg + ntile * 1024, 0, u32x4{a0, a1, b0, b1});
    };
#pragma unroll
    for (int i = 0; i < 2; ++i) {
      const float cv = colsum1(i);
      store_bt(rs4[0] * ac[0][i] + nm4[0] * cv, rs4[1] * ac[1][i] + nm4[1] * cv, (j >> 3) * 32 + (j & 7) * 4 + 2 * wc + i);
    }

    if constexpr (!Z2) {
      float rl[2], nl[2];
#pragma unroll
      for (int m = 0; m < 2; ++m) { rl[m] = xrs[32 * wr4 + 16 * m + c]; nl[m] = xnm[32 * wr4 + 16 * m + c]; }
      u32x4 zp[3];
#pragma unroll
      for (int i = 0; i < 3; ++i) {
        f32x4 cv;
#pragma unroll
        for (int r = 0; r < 4; ++r) { const f32x2 t = *(const f32x2*)(csum + ((wc * 5 + 2 + i) * 16 + 4 * q + r) * 2); cv[r] = t[0] + t[1]; }
#pragma unroll
        for (int m = 0; m < 2; ++m) {
          const f32x4 v = rl[m] * ac[m][2 + i] + nl[m] * cv;
          zp[i][2 * m] = pack2(gelu_tanh(v[0]), gelu_tanh(v[1]));
          zp[i][2 * m + 1] = pack2(gelu_tanh(v[2]), gelu_tanh(v[3]));
        }
      }
      asm volatile("" : "+v"(zp[0]), "+v"(zp[1]), "+v"(zp[2]));
      WAIT_V(0); BAR();
      TSTAMP(5);
      if (w == 0) flag_set(rs, FL_AB + panel * 16 + j, tag, lane);
      const unsigned z1o = WS_Z1 + (unsigned)(panel * 4 + wr4) * (NFT * 1024) + lane * 16;
#pragma unroll
      for (int i = 0; i < 3; ++i)
        if (3 * wc + i < fcnt) st16(rs, z1o + (fstart + 3 * wc + i) * 1024, 0, zp[i]);
      if (w == 0) flag_wait(rs, FL_AB + (ykh * 8 + ((lane >> 1) & 7)) * 16 + ymat * 8 + 2 * yeg + (lane & 1), tag);
      WAIT_V(0); BAR();
      TSTAMP(8);
      if (w == 1) flag_set(rs, FL_Z1 + panel * 8 + part, tag, lane);
    } else {
      f32x4 zv[2][3];
      f32x4 ssum[2] = {}, ssq[2] = {};
#pragma unroll
      for (int i = 0; i < 3; ++i) {
        const float cv = colsum1(2 + i);
        const float valid = (3 * wc + i < fcnt) ? 1.0f : 0.0f;
#pragma unroll
        for (int m = 0; m < 2; ++m) {
          const f32x4 v = rs4[m] * ac[m][2 + i] + nm4[m] * cv;
#pragma unroll
          for (int r = 0; r < 4; ++r) zv[m][i][r] = gelu_tanh(v[r]) * valid;
          ssum[m] += zv[m][i];
          ssq[m] += zv[m][i] * zv[m][i];
        }
      }
      float* zps = (float*)(shm + L_ZPS);
      float* zpq = (float*)(shm + L_ZPQ);
      float* zrs = (float*)(shm + L_ZRS);
      float* znm = (float*)(shm + L_ZNM);
#pragma unroll
      for (int m = 0; m < 2; ++m) {
#pragma unroll
        for (int r = 0; r < 4; ++r) { ssum[m][r] = row16_sum(ssum[m][r]); ssq[m][r] = row16_sum(ssq[m][r]); }
        if (c == 0) {
          *(f32x4*)(zps + wc * 128 + 32 * wr4 + 16 * m + 4 * q) = ssum[m];
          *(f32x4*)(zpq + wc * 128 + 32 * wr4 + 16 * m + 4 * q) = ssq[m];
        }
      }
      WAIT_V(0); WAIT_L0(); BAR();
      TSTAMP(5);
      if (w == 2) flag_set(rs, FL_AB + panel * 16 + j, tag, lane);
      if (w < 2) {
        const unsigned zoff = WS_ZST + (panel * 8 * 128 + tid) * 16;
        granule_store(rs, zoff + part * 2048, zps[tid] + zps[128 + tid], zpq[tid] + zpq[128 + tid], tag);
        for (unsigned spin = 0; spin < SPIN_MAX; ++spin) {
          bool ok = true; float s = 0.f, sq = 0.f;
#pragma unroll
          for (int p = 0; p < 8; ++p) {
            const u32x4 g = ld16(rs, zoff + p * 2048, 0);
            ok = ok & (g[1] == tag[0]) & (g[3] == tag[1]);
            s += __uint_as_float(g[0]); sq += __uint_as_float(g[2]);
          }
          const float mean = s * (1.0f / FH);
          const float var = __builtin_fmaxf(sq * (1.0f / FH) - mean * mean, 0.0f);
          const float rsv = __builtin_amdgcn_rsqf(var + 1e-5f);
          zrs[tid] = rsv; znm[tid] = -mean * rsv;
          if (__all(ok)) break;
          __builtin_amdgcn_s_sleep(1);
        }
      }
      WAIT_L0(); BAR();
      TSTAMP(6);
      f32x4 zr[2], zn[2];
#pragma unroll
      for (int m = 0; m < 2; ++m) {
        zr[m] = *(const f32x4*)(zrs + 32 * wr4 + 16 * m + 4 * q);
        zn[m] = *(const f32x4*)(znm + 32 * wr4 + 16 * m + 4 * q);
      }
#pragma unroll
      for (int i = 0; i < 3; ++i)
        if (3 * wc + i < fcnt) store_bt(zv[0][i] * zr[0] + zn[0], zv[1][i] * zr[1] + zn[1], 64 + fstart + 3 * wc + i);
      WAIT_V(0); BAR();
      TSTAMP(7);
      if (w == 0) {
        flag_set(rs, FL_Z2 + panel * 8 + part, tag, lane);
        flag_wait(rs, lane < 16 ? FL_Z2 + lane * 8 + gpart : FL_Z1 + gpanel * 8 + gpart, tag);
      }
      BAR();
      TSTAMP(8);
    }
  }

  if (prod) {
#pragma unroll
    for (int s = 0; s < P::PF; ++s) issueBB(s, s);
#pragma unroll
    for (int s = P::PF; s < P::NS; ++s) { issueBA(s, s); issueBB(s, s); }
    WAIT_V(P::span(1, P::NS - 1)); BAR();
    constexpr int NPEEL = P::PF > 2 ? P::PF - 2 : 0;
    static_for<0, NPEEL>([&](auto T) {
      constexpr int t = T.value;
      WAIT_V(P::span(t + 2, t + P::NS - 1)); BAR();
      issueBA(t + P::NS, t % P::NS); issueBB(t + P::NS, t % P::NS);
    });
    int slot = NPEEL % P::NS;
    for (int t = NPEEL; t < P::NSTG - P::NS; ++t) {
      WAIT_V((P::NS - 2) * P::NP); BAR();
      issueBA(t + P::NS, slot); issueBB(t + P::NS, slot);
      slot = (slot + 1 == P::NS) ? 0 : slot + 1;
    }
    static_for<0, P::NS>([&](auto I) {
      constexpr int rem = P::NS - 2 - I.value;
      WAIT_V((rem > 0 ? rem : 0) * P::NP); BAR();
    });
    const int rr = lane >> 3;
    const unsigned lo = (unsigned)rr * 1024u + (unsigned)(G * 128 + (((lane & 7) ^ (2 * ((rr >> 1) & 3))) << 4));
#pragma unroll
    for (int k = 0; k < 22; ++k) {
      if (k == 10) BAR();
      int Pc = 4 * k + pw; Pc = Pc < 85 ? Pc : 85;
      glds16o(wout + (size_t)Pc * 8192, lo, shm + Pc * 1024);
    }
    WAIT_V(0); BAR();
  } else {
    f32x4 acc[P::MT][P::NT] = {};
    u32x4 z1[6]; float bs[2];
    if constexpr (Z2) {
#pragma unroll
      for (int n = 0; n < 6; ++n)
        z1[n] = ld16(rs, WS_Z1 + (unsigned)((gpanel * 4 + w) * NFT + gstart + (n < gcnt ? n : gcnt - 1)) * 1024 + lane * 16, 0);
#pragma unroll
      for (int m = 0; m < 2; ++m) bs[m] = bflo(*(const unsigned short*)(bsp + (gpanel * 128 + w * 32 + m * 16 + c) * 2));
    }
    {
      constexpr int MT = P::MT, NT = P::NT;
      bf16x8 fa[2][MT], fb[2][NT];
      int aoff[2];
#pragma unroll
      for (int ks = 0; ks < 2; ++ks) aoff[ks] = ((Z2 ? 32 * w : 128 * wr) + c) * 128 + (((ks * 4 + q) ^ ((c >> 1) & 7)) << 4);
      const int boff = P::BOFF + (Z2 ? 0 : wc * 4096) + lane * 16;
      auto rd = [&](int set, int slot, int ks) {
        const char* sa = shm + slot * P::ABYTES;
        const char* sb = shm + slot * P::BBYTES;
#pragma unroll
        for (int i = 0; i < (MT > NT ? MT : NT); ++i) {
          if (i < NT) fb[set][i] = lds_frag(sb + boff + ks * P::BKS + i * 1024);
          if (i < MT) fa[set][i] = lds_frag(sa + aoff[ks] + i * 2048);
        }
      };
      auto mm = [&](int set) {
#pragma unroll
        for (int m = 0; m < MT; ++m)
#pragma unroll
          for (int n = 0; n < NT; ++n) acc[m][n] = MFMA(fb[set][n], fa[set][m], acc[m][n]);
      };
      auto interleave = [&]() {
#pragma unroll
        for (int i = 0; i < MT + NT; ++i) { __builtin_amdgcn_sched_group_barrier(0x8, 1, 0); __builtin_amdgcn_sched_group_barrier(0x100, 1, 0); }
        __builtin_amdgcn_sched_group_barrier(0x8, MT * NT - MT - NT, 0);
      };
      BAR();
      TSTAMP(9);
      rd(0, 0, 0);
      WAIT_L0(); SCHED();
      int slot = 0;
      for (int t = 0; t < P::NSTG; ++t) {
        rd(1, slot, 1); mm(0); interleave(); SCHED();
        WAIT_L0(); BAR(); SCHED();
        slot = (slot + 1 == P::NS) ? 0 : slot + 1;
        rd(0, slot, 0);
        mm(1); interleave(); SCHED();
      }
    }
    TSTAMP(10);
    if constexpr (Z2) {
#pragma unroll
      for (int m = 0; m < 2; ++m)
#pragma unroll
        for (int p = 0; p < 3; ++p) {
          u32x4 o;
#pragma unroll
          for (int h = 0; h < 2; ++h) {
            const int n = 2 * p + h;
            const unsigned zl = z1[n][2 * m], zh = z1[n][2 * m + 1];
            const f32x4 g = acc[m][n] + bs[m];
            o[2 * h] = pack2(bflo(zl) * g[0], bfhi(zl) * g[1]);
            o[2 * h + 1] = pack2(bflo(zh) * g[2], bfhi(zh) * g[3]);
          }
          if (p == 2 && gcnt < 6) { o[2] = 0; o[3] = 0; }
          st16(rs, WS_H + (unsigned)((gpanel * 8 + 2 * w + m) * 24 + gpart * 3 + p) * 1024 + lane * 16, 0, o);
        }
    } else {
      const unsigned yo = WS_Y + (unsigned)(((ymat * 2 + ykh) * 128 + yp * 16 + wr * 8) * 16 + yeg * 4 + wc * 2) * 1024 + lane * 16;
#pragma unroll
      for (int m = 0; m < 8; ++m)
#pragma unroll
        for (int h = 0; h < 2; ++h) {
          const f32x4 v0 = acc[m][2 * h], v1 = acc[m][2 * h + 1];
          unsigned a0 = pack2(v0[0], v0[1]), a1 = pack2(v0[2], v0[3]), b0 = pack2(v1[0], v1[1]), b1 = pack2(v1[2], v1[3]);
          swap16(a0, b0); swap16(a1, b1);
          st16(rs, yo + m * 16384 + h * 1024, 0, u32x4{a0, a1, b0, b1});
        }
    }
    WAIT_V(0); BAR();
    TSTAMP(11);
    if (w == 0) flag_set(rs, Z2 ? FL_H + gpanel * 8 + gpart : FL_Y + ((ymat * 2 + ykh) * 8 + yp) * 4 + yeg, tag, lane);
    const size_t eo = (size_t)(tb * 64 + w * 16 + c) * 1024 + (G * 64 + 16 * (q & 1) + 8 * (q >> 1)) * 2;
    u32x4 xv[2];
#pragma unroll
    for (int h = 0; h < 2; ++h) xv[h] = *(const u32x4*)(x + eo + h * 64);
    if (w == 0) flag_wait(rs, lane < 8 ? FL_H + (tb >> 1) * 8 + lane : FL_Y + (((lane - 8) & 3) * 8 + (tb >> 2)) * 4 + (G >> 1), tag);
    if (w == 1 && blockIdx.x == 0) {
#pragma unroll
      for (int k = 0; k < 4; ++k) flag_wait(rs, FL_AB + k * 64 + lane, tag);
      if (lane == 0) __builtin_amdgcn_raw_buffer_store_b64(tag, rs, WS_SALT, 0, SC1);
    }
    BAR();
    TSTAMP(12);
    bf16x8 hf[24];
    u32x4 yv[4][2];
#pragma unroll
    for (int kb = 0; kb < 24; ++kb) hf[kb] = __builtin_bit_cast(bf16x8, ld16(rs, WS_H + (unsigned)((tb * 4 + w) * 24 + kb) * 1024 + lane * 16, 0));
#pragma unroll
    for (int h = 0; h < 2; ++h)
#pragma unroll
      for (int p = 0; p < 4; ++p) yv[p][h] = ld16(rs, WS_Y + (unsigned)((p * 128 + tb * 4 + w) * 16 + 2 * G + h) * 1024 + lane * 16, 0);
    int at[4];
    {
      const int r = c >> 2, sg = (2 * (q & 1) + (r >> 1)) & 3;
#pragma unroll
      for (int T = 0; T < 4; ++T) at[T] = (4 * q + r) * 128 + ((T ^ sg) << 5) + (c & 3) * 8;
    }
    f32x4 oc[4] = {};
    bf16x8 wf[3][4];
    auto rdw = [&](auto KB) {
      constexpr int kb = KB.value, pt = kb / 3, pr = kb % 3, ro = (part_start(pt) + 2 * pr) * 2048;
      constexpr bool full = 2 * pr + 1 < part_cnt(pt);
#pragma unroll
      for (int T = 0; T < 4; ++T) {
        const bf16x4 lo = lds_tr(shm + at[T] + ro);
        const bf16x4 hi = full ? lds_tr(shm + at[T] + ro + 2048) : bf16x4{0, 0, 0, 0};
        wf[kb % 3][T] = cat(lo, hi);
      }
    };
    rdw(std::integral_constant<int, 0>{});
    rdw(std::integral_constant<int, 1>{});
    SCHED();
    static_for<0, 24>([&](auto KB) {
      constexpr int kb = KB.value;
      if constexpr (kb + 2 < 24) rdw(std::integral_constant<int, kb + 2>{});
#pragma unroll
      for (int T = 0; T < 4; ++T) oc[T] = MFMA(wf[kb % 3][T], hf[kb], oc[T]);
#pragma unroll
      for (int T = 0; T < 4; ++T) { __builtin_amdgcn_sched_group_barrier(0x8, 1, 0); __builtin_amdgcn_sched_group_barrier(0x100, 2, 0); }
      SCHED();
    });
    TSTAMP(13);
#pragma unroll
    for (int h = 0; h < 2; ++h) {
      float o[8];
#pragma unroll
      for (int r = 0; r < 4; ++r) {
        unsigned a = __float_as_uint(oc[2 * h][r]), b = __float_as_uint(oc[2 * h + 1][r]);
        swap16(a, b);
        o[r] = __uint_as_float(a); o[4 + r] = __uint_as_float(b);
      }
      unsigned res[4];
#pragma unroll
      for (int d = 0; d < 4; ++d) {
        const float yl = (bflo(yv[0][h][d]) + bflo(yv[1][h][d])) - (bflo(yv[2][h][d]) + bflo(yv[3][h][d]));
        const float yh = (bfhi(yv[0][h][d]) + bfhi(yv[1][h][d])) - (bfhi(yv[2][h][d]) + bfhi(yv[3][h][d]));
        res[d] = pack2(bflo(xv[h][d]) + yl * silu(o[2 * d]), bfhi(xv[h][d]) + yh * silu(o[2 * d + 1]));
      }
      __builtin_amdgcn_raw_buffer_store_b128(u32x4{res[0], res[1], res[2], res[3]}, __builtin_amdgcn_make_buffer_rsrc((void*)out, 0, S_ * D_ * 2, 0x00020000), (unsigned)eo + h * 64, 0, OUT_AUX);
    }
  }
}

__global__ __launch_bounds__(512, 2) void kF(const char* __restrict__ x, const char* __restrict__ wsre, const char* __restrict__ wsim,
                                             const char* __restrict__ wdre, const char* __restrict__ wdim, const char* __restrict__ win,
                                             const char* __restrict__ wsp, const char* __restrict__ bsp, const char* __restrict__ wout,
                                             char* __restrict__ ws, char* __restrict__ out) {
  extern __shared__ __attribute__((aligned(1024))) char shm[];
  TSTAMP(0);
  const int xcd = blockIdx.x & 7;
  if (xcd < 4) body<true>(x, wdre, wsp, win, bsp, wout, ws, out, shm);
  else body<false>(x, wdim, (xcd & 2) ? wsim : wsre, win, bsp, wout, ws, out, shm);
  TSTAMP(1);
}

extern "C" void kernel_launch(void* const* d_in, const int* in_sizes, int n_in, void* d_out, int out_size, void* d_ws, size_t ws_size,
                              hipStream_t stream) {
  (void)hipFuncSetAttribute((const void*)kF, hipFuncAttributeMaxDynamicSharedMemorySize, LDS_BYTES);
  kF<<<256, 512, LDS_BYTES, stream>>>((const char*)d_in[0], (const char*)d_in[1], (const char*)d_in[2], (const char*)d_in[3],
                                      (const char*)d_in[4], (const char*)d_in[6], (const char*)d_in[7], (const char*)d_in[8],
                                      (const char*)d_in[9], (char*)d_ws, (char*)d_out);
}
```

```cpp
#define TIMING
#include <hip/hip_runtime.h>
#include <hip/hip_bf16.h>
#include <stdint.h>
#include <type_traits>

typedef short bf16x8 __attribute__((ext_vector_type(8)));
typedef short bf16x4 __attribute__((ext_vector_type(4)));
typedef short v4i16_t __attribute__((ext_vector_type(4)));
typedef float f32x4 __attribute__((ext_vector_type(4)));
typedef float f32x2 __attribute__((ext_vector_type(2)));
typedef unsigned u32x4 __attribute__((ext_vector_type(4)));
typedef unsigned u32x2 __attribute__((ext_vector_type(2)));
typedef __bf16 bf16v2 __attribute__((ext_vector_type(2)));
typedef const __attribute__((address_space(1))) void* gptr_t;
typedef __attribute__((address_space(3))) void* lptr_t;
typedef __amdgpu_buffer_rsrc_t rsrc_t;

#define WAIT_V(n) asm volatile("s_waitcnt vmcnt(%0)" ::"n"(n) : "memory")
#define WAIT_L0() asm volatile("s_waitcnt lgkmcnt(0)" ::: "memory")
#define BAR() do { asm volatile("" ::: "memory"); __builtin_amdgcn_s_barrier(); asm volatile("" ::: "memory"); } while (0)
#define SCHED() __builtin_amdgcn_sched_barrier(0)
#define MFMA(a, b, c) __builtin_amdgcn_mfma_f32_16x16x32_bf16(a, b, c, 0, 0, 0)

#define OUT_AUX -1
constexpr int S_ = 2048, D_ = 512, F_ = 1376, FH = 688, NFT = 43;
constexpr int NT_ALL = 112;
constexpr unsigned WS_BIMG = 0;
constexpr unsigned WS_Z1 = 8u << 20;
constexpr unsigned WS_H = 12u << 20;
constexpr unsigned WS_Y = 16u << 20;
constexpr unsigned WS_XST = 24u << 20;
constexpr unsigned WS_ZST = WS_XST + 65536;
constexpr unsigned WS_FLG = WS_ZST + 262144;
constexpr unsigned WS_SALT = WS_FLG + 131072;
constexpr unsigned WS_END = 25u << 20;
constexpr unsigned WS_DBG = 26u << 20;
constexpr int FL_AB = 0, FL_Z2 = 256, FL_Z1 = 384, FL_H = 512, FL_Y = 640;

template <int I, int N, class F>
__device__ __forceinline__ void static_for(F&& f) {
  if constexpr (I < N) { f(std::integral_constant<int, I>{}); static_for<I + 1, N>(f); }
}
__device__ __forceinline__ unsigned pack2(float a, float b) {
  return __builtin_bit_cast(unsigned, __builtin_convertvector(f32x2{a, b}, bf16v2));
}
__device__ __forceinline__ float bflo(unsigned u) { return __uint_as_float(u << 16); }
__device__ __forceinline__ float bfhi(unsigned u) { return __uint_as_float(u & 0xffff0000u); }
template <int AUX = 0>
__device__ __forceinline__ void glds16o(const char* ubase, unsigned off, char* l) {
  asm volatile("" : "+v"(off));
  __builtin_amdgcn_global_load_lds((gptr_t)(ubase + off), (lptr_t)l, 16, 0, AUX);
}
__device__ __forceinline__ void swap16(unsigned& a, unsigned& b) {
  auto r = __builtin_amdgcn_permlane16_swap(a, b, false, false);
  a = r[0]; b = r[1];
}
template <int CTRL>
__device__ __forceinline__ float dpp_add(float v) {
  return v + __int_as_float(__builtin_amdgcn_update_dpp(0, __float_as_int(v), CTRL, 0xf, 0xf, true));
}
__device__ __forceinline__ float row16_sum(float v) {
  v = dpp_add<0x128>(v); v = dpp_add<0x124>(v); v = dpp_add<0x122>(v); v = dpp_add<0x121>(v);
  return v;
}
__device__ __forceinline__ float wave_sum(float v) {
  v = row16_sum(v);
  const int i = __float_as_int(v);
  return __int_as_float(__builtin_amdgcn_readlane(i, 0)) + __int_as_float(__builtin_amdgcn_readlane(i, 16)) +
         __int_as_float(__builtin_amdgcn_readlane(i, 32)) + __int_as_float(__builtin_amdgcn_readlane(i, 48));
}
__device__ __forceinline__ float gelu_tanh(float v) {
  const float c0 = -2.3022081986f, c1 = -0.1029432397f;
  const float e = __builtin_amdgcn_exp2f(v * __builtin_fmaf(v * v, c1, c0));
  return v * __builtin_amdgcn_rcpf(1.0f + e);
}
__device__ __forceinline__ float silu(float v) {
  const float e = __builtin_amdgcn_exp2f(v * -1.4426950409f);
  return v * __builtin_amdgcn_rcpf(1.0f + e);
}
__device__ __forceinline__ bf16x8 lds_frag(const char* p) { return *(const bf16x8*)p; }
__device__ __forceinline__ bf16x4 lds_tr(const char* p) {
  return __builtin_bit_cast(bf16x4, __builtin_amdgcn_ds_read_tr16_b64_v4i16((__attribute__((address_space(3))) v4i16_t*)p));
}
__device__ __forceinline__ bf16x8 cat(bf16x4 a, bf16x4 b) { return __builtin_shufflevector(a, b, 0, 1, 2, 3, 4, 5, 6, 7); }

constexpr int SC1 = 16;
__device__ __forceinline__ void st16(rsrc_t rs, unsigned voff, unsigned soff, u32x4 v) { __builtin_amdgcn_raw_buffer_store_b128(v, rs, voff, soff, SC1); }
__device__ __forceinline__ u32x4 ld16(rsrc_t rs, unsigned voff, unsigned soff) { return __builtin_amdgcn_raw_buffer_load_b128(rs, voff, soff, SC1); }
__device__ __forceinline__ void granule_store(rsrc_t rs, unsigned off, float v0, float v1, u32x2 tag) {
  st16(rs, off, 0, u32x4{__float_as_uint(v0), tag[0], __float_as_uint(v1), tag[1]});
}
constexpr unsigned SPIN_MAX = 1u << 18;
__device__ __forceinline__ void flag_set(rsrc_t rs, int idx, u32x2 tag, int lane) {
  if (lane == 0) __builtin_amdgcn_raw_buffer_store_b64(tag, rs, WS_FLG + idx * 128, 0, SC1);
}
__device__ __forceinline__ void flag_wait(rsrc_t rs, int idx, u32x2 tag) {
  for (unsigned spin = 0; spin < SPIN_MAX; ++spin) {
    const u32x2 g = __builtin_amdgcn_raw_buffer_load_b64(rs, WS_FLG + idx * 128, 0, SC1);
    if (__all((g[0] == tag[0]) & (g[1] == tag[1]))) break;
    __builtin_amdgcn_s_sleep(1);
  }
  asm volatile("" ::: "memory");
}

#define TSTAMPW(slot, wv) do { if (__builtin_amdgcn_readfirstlane(threadIdx.x >> 6) == (wv)) { unsigned long long* tp_ = (unsigned long long*)(ws + WS_DBG) + blockIdx.x * 16 + (slot); unsigned long long tv_ = __builtin_amdgcn_s_memrealtime(); asm volatile("global_store_dwordx2 %0, %1, off" :: "v"(tp_), "v"(tv_) : "memory"); } } while (0)
#define TSTAMP(slot) TSTAMPW(slot, 1)

__device__ __forceinline__ constexpr int part_start(int p) { return p < 3 ? 6 * p : 18 + 5 * (p - 3); }
__device__ __forceinline__ constexpr int part_cnt(int p) { return p < 3 ? 6 : 5; }

constexpr int A_STAGE = 36864, A_NS = 4, A_XO = 0, A_DO = 16384, A_WO = 24576;
constexpr int L_HO = 69632;
constexpr int L_CS = A_NS * A_STAGE;
constexpr int L_XRS = L_CS + 2048;
constexpr int L_XNM = L_XRS + 512;
constexpr int L_ZPS = L_XNM + 512;
constexpr int L_ZPQ = L_ZPS + 1024;
constexpr int L_ZRS = L_ZPQ + 1024;
constexpr int L_ZNM = L_ZRS + 512;
constexpr int LDS_BYTES = L_ZNM + 512;

template <bool G>
struct PB {
  static constexpr int NA = G ? 4 : 8, NB = G ? 3 : 4, NP = NA + NB;
  static constexpr int NS = G ? 5 : 3, NSTG = G ? 32 : 16, PF = G ? 4 : 2;
  static constexpr int ABYTES = NA * 4096, BBYTES = NB * 4096, BOFF = G ? 81920 : 98304;
  static constexpr int MT = G ? 2 : 8, NT = G ? 6 : 4, BKS = BBYTES / 2;
  static constexpr int cnt(int s) { return s >= NSTG ? 0 : (s < PF ? NB : NP); }
  static constexpr int span(int s0, int s1) { return s0 > s1 ? 0 : cnt(s0) + span(s0 + 1, s1); }
};

template <bool Z2>
__device__ __forceinline__ void body(const char* __restrict__ x, const char* __restrict__ wd, const char* __restrict__ wsq,
                                     const char* __restrict__ win, const char* __restrict__ bsp, const char* __restrict__ wout,
                                     char* __restrict__ ws, char* __restrict__ out, char* shm) {
  typedef PB<Z2> P;
  const int tid = threadIdx.x, w = __builtin_amdgcn_readfirstlane(tid >> 6), lane = tid & 63;
  const int c = lane & 15, q = lane >> 4;
  const int xcd = blockIdx.x & 7, bi = blockIdx.x >> 3;
  const bool prod = w >= 4;
  const int pw = w & 3, wr = pw >> 1, wc = pw & 1;
  const int panel = (xcd & 1) * 8 + (bi >> 2), j = (xcd >> 1) * 4 + (bi & 3);
  const int part = j & 7, fstart = part_start(part), fcnt = part_cnt(part);
  const int gpanel = xcd * 4 + (bi >> 3), gpart = bi & 7, gstart = part_start(gpart), gcnt = part_cnt(gpart);
  const int ymat = (xcd >> 1) & 1, ykh = xcd & 1, yp = bi >> 2, yeg = bi & 3;
  const int tb = xcd * 4 + (bi >> 3), G = bi & 7;

  const rsrc_t rs = __builtin_amdgcn_make_buffer_rsrc((void*)ws, 0, WS_END, 0x00020000);
  u32x2 tag = {0, 0};
  f32x4 ac[2][5];
  unsigned oa[P::NA], ob[P::NB];
  const char* Ab = wsq + (Z2 ? (size_t)gpanel * 128 * 4096 : (size_t)yp * 256 * 4096 + ykh * 2048);
  const char* Bb = ws + WS_BIMG + (Z2 ? 0 : (size_t)ykh * 32 * (NT_ALL * 1024));
  auto issueBA = [&](int t, int slot) {
    const char* ab = Ab + t * 128;
    asm volatile("" : "+s"(ab));
#pragma unroll
    for (int i = 0; i < P::NA; ++i) glds16o(ab, oa[i], shm + slot * P::ABYTES + (pw * P::NA + i) * 1024);
  };
  auto issueBB = [&](int t, int slot) {
    const char* bb = Bb + (size_t)t * (2 * NT_ALL * 1024);
    asm volatile("" : "+s"(bb));
#pragma unroll
    for (int i = 0; i < P::NB; ++i) glds16o<SC1>(bb, ob[i], shm + P::BOFF + slot * P::BBYTES + (pw * P::NB + i) * 1024);
  };

  float* csum = (float*)(shm + L_CS);
  float* xrs = (float*)(shm + L_XRS);
  float* xnm = (float*)(shm + L_XNM);
  if (prod) {
    unsigned ox[4], od[2], ow[3];
#pragma unroll
    for (int i = 0; i < 4; ++i) {
      const int row = 8 * (pw * 4 + i) + (lane >> 3);
      ox[i] = (unsigned)(panel * 128 + row) * 1024u + (unsigned)(((lane & 7) ^ ((row >> 1) & 7)) << 4);
    }
#pragma unroll
    for (int i = 0; i < 2; ++i) {
      const int row = 8 * (pw * 2 + i) + (lane >> 3);
      od[i] = (unsigned)((j & 7) * 64 + row) * 1024u + (unsigned)(((lane & 7) ^ ((row >> 1) & 7)) << 4);
    }
#pragma unroll
    for (int i = 0; i < 3; ++i) {
      const int o = (pw * 3 + i) * 1024 + lane * 16, row = o / 192, ch = (o % 192) >> 4;
      const int u = (ch >> 1) ^ ((row >> 3) & 1);
      const int ft = fstart + (u < fcnt ? u : fcnt - 1);
      ow[i] = (unsigned)row * 2752u + (unsigned)((Z2 ? FH * 2 : 0) + ft * 32 + (ch & 1) * 16);
    }
    auto issue = [&](int t) {
      char* st = shm + (t % A_NS) * A_STAGE;
      const char* xb = x + t * 128;
      const char* db = wd + t * 128;
      const char* wb = win + (size_t)t * (64 * 2752);
      asm volatile("" : "+s"(xb), "+s"(db), "+s"(wb));
#pragma unroll
      for (int i = 0; i < 4; ++i) glds16o(xb, ox[i], st + A_XO + (pw * 4 + i) * 1024);
#pragma unroll
      for (int i = 0; i < 2; ++i) glds16o(db, od[i], st + A_DO + (pw * 2 + i) * 1024);
#pragma unroll
      for (int i = 0; i < 3; ++i) glds16o(wb, ow[i], st + A_WO + (pw * 3 + i) * 1024);
    };
#pragma unroll
    for (int t = 0; t < A_NS; ++t) issue(t);
#pragma unroll
    for (int i = 0; i < P::NA; ++i) {
      const int row = (pw * P::NA + i) * 8 + (lane >> 3);
      oa[i] = (unsigned)row * 4096u + (unsigned)(((lane & 7) ^ ((row >> 1) & 7)) << 4);
    }
#pragma unroll
    for (int i = 0; i < P::NB; ++i) {
      const int n = (pw & 1) * P::NB + i;
      const int nt = Z2 ? 64 + gstart + (n < gcnt ? n : gcnt - 1) : ymat * 32 + yeg * 8 + n;
      ob[i] = (unsigned)(((pw >> 1) * NT_ALL + nt) * 1024 + lane * 16);
    }
    WAIT_V(27); BAR();
    static_for<0, 7>([&](auto T) {
      constexpr int t = T.value;
      WAIT_V(t <= 4 ? 18 : (t == 5 ? 9 : P::PF * P::NA));
      BAR();
      if constexpr (t + A_NS < 8) issue(t + A_NS);
      if constexpr (t == 5) {
#pragma unroll
        for (int s = 0; s < P::PF; ++s) issueBA(s, s);
      }
    });
  } else {
    u32x2 salt; u32x4 xr0, xr1;
    {
      const char* ps = ws + WS_SALT;
      const char* pr = x + (size_t)(panel * 128 + 8 * j + 2 * w) * 1024 + lane * 16;
      asm volatile("global_load_dwordx2 %0, %1, off sc1" : "=&v"(salt) : "v"(ps) : "memory");
      asm volatile("global_load_dwordx4 %0, %1, off" : "=&v"(xr0) : "v"(pr) : "memory");
      asm volatile("global_load_dwordx4 %0, %1, off offset:1024" : "=&v"(xr1) : "v"(pr) : "memory");
    }
    f32x4 acc[4][5] = {};
    f32x4 cs[5] = {};
    bf16x8 fx[2][4], fd[2][2], fw[2][3];
    const bf16x8 ones = {0x3F80, 0x3F80, 0x3F80, 0x3F80, 0x3F80, 0x3F80, 0x3F80, 0x3F80};
    int ax[2], ad[2], aw[3];
#pragma unroll
    for (int ks = 0; ks < 2; ++ks) {
      const int sw = ((ks * 4 + q) ^ ((c >> 1) & 7)) << 4;
      ax[ks] = A_XO + (64 * wr + c) * 128 + sw;
      ad[ks] = A_DO + (32 * wc + c) * 128 + sw;
    }
#pragma unroll
    for (int i = 0; i < 3; ++i) aw[i] = A_WO + (8 * q + (c >> 2)) * 192 + (((3 * wc + i) ^ (q & 1)) << 5) + (c & 3) * 8;
    asm volatile("s_waitcnt vmcnt(0)" : "+v"(salt), "+v"(xr0), "+v"(xr1)::"memory");
    {
      const unsigned long long tag64 = (((unsigned long long)salt[1] << 32) | salt[0]) + 0x9E3779B97F4A7C15ull;
      tag = u32x2{(unsigned)tag64, (unsigned)(tag64 >> 32)};
      float v[2][8], mean[2], ss[2];
#pragma unroll
      for (int i = 0; i < 4; ++i) { v[0][2 * i] = bflo(xr0[i]); v[0][2 * i + 1] = bfhi(xr0[i]); v[1][2 * i] = bflo(xr1[i]); v[1][2 * i + 1] = bfhi(xr1[i]); }
#pragma unroll
      for (int r = 0; r < 2; ++r)
        mean[r] = wave_sum(((v[r][0] + v[r][1]) + (v[r][2] + v[r][3])) + ((v[r][4] + v[r][5]) + (v[r][6] + v[r][7]))) * (1.0f / D_);
#pragma unroll
      for (int r = 0; r < 2; ++r) {
        ss[r] = 0.f;
#pragma unroll
        for (int i = 0; i < 8; ++i) { const float d = v[r][i] - mean[r]; ss[r] = __builtin_fmaf(d, d, ss[r]); }
        ss[r] = __builtin_amdgcn_rsqf(wave_sum(ss[r]) * (1.0f / D_) + 1e-5f);
      }
      const int r = lane & 1;
      const float rsv = r ? ss[1] : ss[0], mv = r ? mean[1] : mean[0];
      if (lane < 2) granule_store(rs, WS_XST + (panel * 128 + 8 * j + 2 * w + r) * 16, rsv, -mv * rsv, tag);
    }
    auto rd = [&](int set, int t, int ks) {
      const char* st = shm + (t % A_NS) * A_STAGE;
#pragma unroll
      for (int i = 0; i < 2; ++i) fd[set][i] = lds_frag(st + ad[ks] + i * 2048);
#pragma unroll
      for (int m = 0; m < 4; ++m) fx[set][m] = lds_frag(st + ax[ks] + m * 2048);
#pragma unroll
      for (int i = 0; i < 3; ++i) fw[set][i] = cat(lds_tr(st + aw[i] + ks * 6144), lds_tr(st + aw[i] + ks * 6144 + 768));
    };
    auto mm = [&](int set) {
#pragma unroll
      for (int i = 0; i < 2; ++i)
#pragma unroll
        for (int m = 0; m < 4; ++m) acc[m][i] = MFMA(fx[set][m], fd[set][i], acc[m][i]);
#pragma unroll
      for (int i = 0; i < 3; ++i)
#pragma unroll
        for (int m = 0; m < 4; ++m)
          acc[m][2 + i] = Z2 ? MFMA(fx[set][m], fw[set][i], acc[m][2 + i]) : MFMA(fw[set][i], fx[set][m], acc[m][2 + i]);
    };
    auto mmcs = [&](int set) {
#pragma unroll
      for (int i = 0; i < 2; ++i) cs[i] = MFMA(ones, fd[set][i], cs[i]);
#pragma unroll
      for (int i = 0; i < 3; ++i) cs[2 + i] = Z2 ? MFMA(ones, fw[set][i], cs[2 + i]) : MFMA(fw[set][i], ones, cs[2 + i]);
    };
    auto interleave = [&]() {
#pragma unroll
      for (int i = 0; i < 12; ++i) { __builtin_amdgcn_sched_group_barrier(0x8, 1, 0); __builtin_amdgcn_sched_group_barrier(0x100, 1, 0); }
      __builtin_amdgcn_sched_group_barrier(0x8, 8, 0);
    };
    const unsigned gxo = WS_XST + (panel * 128 + 32 * w + (lane & 31)) * 16;
    u32x4 gx;
    BAR();
    TSTAMP(2);
    rd(0, 0, 0);
    WAIT_L0(); SCHED();
    static_for<0, 8>([&](auto T) {
      constexpr int t = T.value;
      rd(1, t, 1); mm(0); interleave(); SCHED();
      if (wr == 0) mmcs(0);
      if constexpr (t < 7) {
        WAIT_L0(); BAR(); SCHED();
        if constexpr (t == 5) gx = ld16(rs, gxo, 0);
        rd(0, t + 1, 0); mm(1); interleave(); SCHED();
      } else {
        mm(1);
      }
      if (wr == 1) mmcs(1);
    });
    TSTAMP(3);
#pragma unroll
    for (int m = 0; m < 2; ++m)
#pragma unroll
      for (int i = 0; i < 5; ++i) {
        *(f32x4*)(shm + L_HO + (pw * 10 + m * 5 + i) * 1024 + lane * 16) = acc[2 + m][i];
        ac[m][i] = acc[m][i];
      }
#pragma unroll
    for (int i = 0; i < 5; ++i) {
      if (i < 2 || Z2) { if (q == 0) csum[((wc * 5 + i) * 16 + c) * 2 + wr] = cs[i][0]; }
      else if (c == 0) {
#pragma unroll
        for (int r = 0; r < 4; ++r) csum[((wc * 5 + i) * 16 + 4 * q + r) * 2 + wr] = cs[i][r];
      }
    }
    for (unsigned spin = 0; spin < SPIN_MAX; ++spin) {
      if (__all((gx[1] == tag[0]) & (gx[3] == tag[1]))) break;
      __builtin_amdgcn_s_sleep(1);
      gx = ld16(rs, gxo, 0);
    }
    if (lane < 32) { xrs[32 * w + lane] = __uint_as_float(gx[0]); xnm[32 * w + lane] = __uint_as_float(gx[2]); }
    WAIT_L0();
  }
  BAR();
  TSTAMP(4);

  {
    const int wr4 = 2 * wr + (w >> 2);
    if (prod) {
#pragma unroll
      for (int m = 0; m < 2; ++m)
#pragma unroll
        for (int i = 0; i < 5; ++i) ac[m][i] = *(const f32x4*)(shm + L_HO + (pw * 10 + m * 5 + i) * 1024 + lane * 16);
    }
    const unsigned bimg = WS_BIMG + (unsigned)(panel * 4 + wr4) * (NT_ALL * 1024) + (c + 16 * (((q & 1) << 1) | (q >> 1))) * 16;
    f32x4 rs4[2], nm4[2];
#pragma unroll
    for (int m = 0; m < 2; ++m) {
      rs4[m] = *(const f32x4*)(xrs + 32 * wr4 + 16 * m + 4 * q);
      nm4[m] = *(const f32x4*)(xnm + 32 * wr4 + 16 * m + 4 * q);
    }
    auto colsum1 = [&](int i) { const f32x2 t = *(const f32x2*)(csum + ((wc * 5 + i) * 16 + c) * 2); return t[0] + t[1]; };
    auto store_bt = [&](const f32x4& v0, const f32x4& v1, int ntile) {
      unsigned a0 = pack2(v0[0], v0[1]), a1 = pack2(v0[2], v0[3]), b0 = pack2(v1[0], v1[1]), b1 = pack2(v1[2], v1[3]);
      swap16(a0, b0); swap16(a1, b1);
      st16(rs, bimg + ntile * 1024, 0, u32x4{a0, a1, b0, b1});
    };
#pragma unroll
    for (int i = 0; i < 2; ++i) {
      const float cv = colsum1(i);
      store_bt(rs4[0] * ac[0][i] + nm4[0] * cv, rs4[1] * ac[1][i] + nm4[1] * cv, (j >> 3) * 32 + (j & 7) * 4 + 2 * wc + i);
    }

    if constexpr (!Z2) {
      float rl[2], nl[2];
#pragma unroll
      for (int m = 0; m < 2; ++m) { rl[m] = xrs[32 * wr4 + 16 * m + c]; nl[m] = xnm[32 * wr4 + 16 * m + c]; }
      u32x4 zp[3];
#pragma unroll
      for (int i = 0; i < 3; ++i) {
        f32x4 cv;
#pragma unroll
        for (int r = 0; r < 4; ++r) { const f32x2 t = *(const f32x2*)(csum + ((wc * 5 + 2 + i) * 16 + 4 * q + r) * 2); cv[r] = t[0] + t[1]; }
#pragma unroll
        for (int m = 0; m < 2; ++m) {
          const f32x4 v = rl[m] * ac[m][2 + i] + nl[m] * cv;
          zp[i][2 * m] = pack2(gelu_tanh(v[0]), gelu_tanh(v[1]));
          zp[i][2 * m + 1] = pack2(gelu_tanh(v[2]), gelu_tanh(v[3]));
        }
      }
      asm volatile("" : "+v"(zp[0]), "+v"(zp[1]), "+v"(zp[2]));
      WAIT_V(0); BAR();
      TSTAMP(5);
      if (w == 0) flag_set(rs, FL_AB + panel * 16 + j, tag, lane);
      const unsigned z1o = WS_Z1 + (unsigned)(panel * 4 + wr4) * (NFT * 1024) + lane * 16;
#pragma unroll
      for (int i = 0; i < 3; ++i)
        if (3 * wc + i < fcnt) st16(rs, z1o + (fstart + 3 * wc + i) * 1024, 0, zp[i]);
      if (w == 0) flag_wait(rs, FL_AB + (ykh * 8 + ((lane >> 1) & 7)) * 16 + ymat * 8 + 2 * yeg + (lane & 1), tag);
      WAIT_V(0); BAR();
      TSTAMP(8);
      if (w == 1) flag_set(rs, FL_Z1 + panel * 8 + part, tag, lane);
    } else {
      f32x4 zv[2][3];
      f32x4 ssum[2] = {}, ssq[2] = {};
#pragma unroll
      for (int i = 0; i < 3; ++i) {
        const float cv = colsum1(2 + i);
        const float valid = (3 * wc + i < fcnt) ? 1.0f : 0.0f;
#pragma unroll
        for (int m = 0; m < 2; ++m) {
          const f32x4 v = rs4[m] * ac[m][2 + i] + nm4[m] * cv;
#pragma unroll
          for (int r = 0; r < 4; ++r) zv[m][i][r] = gelu_tanh(v[r]) * valid;
          ssum[m] += zv[m][i];
          ssq[m] += zv[m][i] * zv[m][i];
        }
      }
      float* zps = (float*)(shm + L_ZPS);
      float* zpq = (float*)(shm + L_ZPQ);
      float* zrs = (float*)(shm + L_ZRS);
      float* znm = (float*)(shm + L_ZNM);
#pragma unroll
      for (int m = 0; m < 2; ++m) {
#pragma unroll
        for (int r = 0; r < 4; ++r) { ssum[m][r] = row16_sum(ssum[m][r]); ssq[m][r] = row16_sum(ssq[m][r]); }
        if (c == 0) {
          *(f32x4*)(zps + wc * 128 + 32 * wr4 + 16 * m + 4 * q) = ssum[m];
          *(f32x4*)(zpq + wc * 128 + 32 * wr4 + 16 * m + 4 * q) = ssq[m];
        }
      }
      WAIT_V(0); WAIT_L0(); BAR();
      TSTAMP(5);
      if (w == 2) flag_set(rs, FL_AB + panel * 16 + j, tag, lane);
      if (w < 2) {
        const unsigned zoff = WS_ZST + (panel * 8 * 128 + tid) * 16;
        granule_store(rs, zoff + part * 2048, zps[tid] + zps[128 + tid], zpq[tid] + zpq[128 + tid], tag);
        for (unsigned spin = 0; spin < SPIN_MAX; ++spin) {
          bool ok = true; float s = 0.f, sq = 0.f;
#pragma unroll
          for (int p = 0; p < 8; ++p) {
            const u32x4 g = ld16(rs, zoff + p * 2048, 0);
            ok = ok & (g[1] == tag[0]) & (g[3] == tag[1]);
            s += __uint_as_float(g[0]); sq += __uint_as_float(g[2]);
          }
          const float mean = s * (1.0f / FH);
          const float var = __builtin_fmaxf(sq * (1.0f / FH) - mean * mean, 0.0f);
          const float rsv = __builtin_amdgcn_rsqf(var + 1e-5f);
          zrs[tid] = rsv; znm[tid] = -mean * rsv;
          if (__all(ok)) break;
          __builtin_amdgcn_s_sleep(1);
        }
      }
      WAIT_L0(); BAR();
      TSTAMP(6);
      f32x4 zr[2], zn[2];
#pragma unroll
      for (int m = 0; m < 2; ++m) {
        zr[m] = *(const f32x4*)(zrs + 32 * wr4 + 16 * m + 4 * q);
        zn[m] = *(const f32x4*)(znm + 32 * wr4 + 16 * m + 4 * q);
      }
#pragma unroll
      for (int i = 0; i < 3; ++i)
        if (3 * wc + i < fcnt) store_bt(zv[0][i] * zr[0] + zn[0], zv[1][i] * zr[1] + zn[1], 64 + fstart + 3 * wc + i);
      WAIT_V(0); BAR();
      TSTAMP(7);
      if (w == 0) {
        flag_set(rs, FL_Z2 + panel * 8 + part, tag, lane);
        flag_wait(rs, lane < 16 ? FL_Z2 + lane * 8 + gpart : FL_Z1 + gpanel * 8 + gpart, tag);
      }
      BAR();
      TSTAMP(8);
    }
  }

  if (prod) {
#pragma unroll
    for (int s = 0; s < P::PF; ++s) issueBB(s, s);
#pragma unroll
    for (int s = P::PF; s < P::NS; ++s) { issueBA(s, s); issueBB(s, s); }
    WAIT_V(P::span(1, P::NS - 1)); BAR();
    constexpr int NPEEL = P::PF > 2 ? P::PF - 2 : 0;
    static_for<0, NPEEL>([&](auto T) {
      constexpr int t = T.value;
      WAIT_V(P::span(t + 2, t + P::NS - 1)); BAR();
      issueBA(t + P::NS, t % P::NS); issueBB(t + P::NS, t % P::NS);
    });
    int slot = NPEEL % P::NS;
    for (int t = NPEEL; t < P::NSTG - P::NS; ++t) {
      WAIT_V((P::NS - 2) * P::NP); BAR();
      issueBA(t + P::NS, slot); issueBB(t + P::NS, slot);
      slot = (slot + 1 == P::NS) ? 0 : slot + 1;
    }
    static_for<0, P::NS>([&](auto I) {
      constexpr int rem = P::NS - 2 - I.value;
      WAIT_V((rem > 0 ? rem : 0) * P::NP); BAR();
    });
    const int rr = lane >> 3;
    const unsigned lo = (unsigned)rr * 1024u + (unsigned)(G * 128 + (((lane & 7) ^ (2 * ((rr >> 1) & 3))) << 4));
#pragma unroll
    for (int k = 0; k < 22; ++k) {
      if (k == 10) BAR();
      int Pc = 4 * k + pw; Pc = Pc < 85 ? Pc : 85;
      glds16o(wout + (size_t)Pc * 8192, lo, shm + Pc * 1024);
    }
    WAIT_V(0); BAR();
  } else {
    f32x4 acc[P::MT][P::NT] = {};
    u32x4 z1[6]; float bs[2];
    if constexpr (Z2) {
#pragma unroll
      for (int n = 0; n < 6; ++n)
        z1[n] = ld16(rs, WS_Z1 + (unsigned)((gpanel * 4 + w) * NFT + gstart + (n < gcnt ? n : gcnt - 1)) * 1024 + lane * 16, 0);
#pragma unroll
      for (int m = 0; m < 2; ++m) bs[m] = bflo(*(const unsigned short*)(bsp + (gpanel * 128 + w * 32 + m * 16 + c) * 2));
    }
    {
      constexpr int MT = P::MT, NT = P::NT;
      bf16x8 fa[2][MT], fb[2][NT];
      int aoff[2];
#pragma unroll
      for (int ks = 0; ks < 2; ++ks) aoff[ks] = ((Z2 ? 32 * w : 128 * wr) + c) * 128 + (((ks * 4 + q) ^ ((c >> 1) & 7)) << 4);
      const int boff = P::BOFF + (Z2 ? 0 : wc * 4096) + lane * 16;
      auto rd = [&](int set, int slot, int ks) {
        const char* sa = shm + slot * P::ABYTES;
        const char* sb = shm + slot * P::BBYTES;
#pragma unroll
        for (int i = 0; i < (MT > NT ? MT : NT); ++i) {
          if (i < NT) fb[set][i] = lds_frag(sb + boff + ks * P::BKS + i * 1024);
          if (i < MT) fa[set][i] = lds_frag(sa + aoff[ks] + i * 2048);
        }
      };
      auto mm = [&](int set) {
#pragma unroll
        for (int m = 0; m < MT; ++m)
#pragma unroll
          for (int n = 0; n < NT; ++n) acc[m][n] = MFMA(fb[set][n], fa[set][m], acc[m][n]);
      };
      auto interleave = [&]() {
#pragma unroll
        for (int i = 0; i < MT + NT; ++i) { __builtin_amdgcn_sched_group_barrier(0x8, 1, 0); __builtin_amdgcn_sched_group_barrier(0x100, 1, 0); }
        __builtin_amdgcn_sched_group_barrier(0x8, MT * NT - MT - NT, 0);
      };
      BAR();
      TSTAMP(9);
      rd(0, 0, 0);
      WAIT_L0(); SCHED();
      int slot = 0;
      for (int t = 0; t < P::NSTG; ++t) {
        rd(1, slot, 1); mm(0); interleave(); SCHED();
        WAIT_L0(); BAR(); SCHED();
        slot = (slot + 1 == P::NS) ? 0 : slot + 1;
        rd(0, slot, 0);
        mm(1); interleave(); SCHED();
      }
    }
    TSTAMP(10);
    if constexpr (Z2) {
#pragma unroll
      for (int m = 0; m < 2; ++m)
#pragma unroll
        for (int p = 0; p < 3; ++p) {
          u32x4 o;
#pragma unroll
          for (int h = 0; h < 2; ++h) {
            const int n = 2 * p + h;
            const unsigned zl = z1[n][2 * m], zh = z1[n][2 * m + 1];
            const f32x4 g = acc[m][n] + bs[m];
            o[2 * h] = pack2(bflo(zl) * g[0], bfhi(zl) * g[1]);
            o[2 * h + 1] = pack2(bflo(zh) * g[2], bfhi(zh) * g[3]);
          }
          if (p == 2 && gcnt < 6) { o[2] = 0; o[3] = 0; }
          st16(rs, WS_H + (unsigned)((gpanel * 8 + 2 * w + m) * 24 + gpart * 3 + p) * 1024 + lane * 16, 0, o);
        }
    } else {
      const unsigned yo = WS_Y + (unsigned)(((ymat * 2 + ykh) * 128 + yp * 16 + wr * 8) * 16 + yeg * 4 + wc * 2) * 1024 + lane * 16;
#pragma unroll
      for (int m = 0; m < 8; ++m)
#pragma unroll
        for (int h = 0; h < 2; ++h) {
          const f32x4 v0 = acc[m][2 * h], v1 = acc[m][2 * h + 1];
          unsigned a0 = pack2(v0[0], v0[1]), a1 = pack2(v0[2], v0[3]), b0 = pack2(v1[0], v1[1]), b1 = pack2(v1[2], v1[3]);
          swap16(a0, b0); swap16(a1, b1);
          st16(rs, yo + m * 16384 + h * 1024, 0, u32x4{a0, a1, b0, b1});
        }
    }
    WAIT_V(0); BAR();
    TSTAMP(11);
    if (w == 0) flag_set(rs, Z2 ? FL_H + gpanel * 8 + gpart : FL_Y + ((ymat * 2 + ykh) * 8 + yp) * 4 + yeg, tag, lane);
    const size_t eo = (size_t)(tb * 64 + w * 16 + c) * 1024 + (G * 64 + 16 * (q & 1) + 8 * (q >> 1)) * 2;
    u32x4 xv[2];
#pragma unroll
    for (int h = 0; h < 2; ++h) xv[h] = *(const u32x4*)(x + eo + h * 64);
    if (w == 0) flag_wait(rs, lane < 8 ? FL_H + (tb >> 1) * 8 + lane : FL_Y + (((lane - 8) & 3) * 8 + (tb >> 2)) * 4 + (G >> 1), tag);
    if (w == 1 && blockIdx.x == 0) {
#pragma unroll
      for (int k = 0; k < 4; ++k) flag_wait(rs, FL_AB + k * 64 + lane, tag);
      if (lane == 0) __builtin_amdgcn_raw_buffer_store_b64(tag, rs, WS_SALT, 0, SC1);
    }
    BAR();
    TSTAMP(12);
    bf16x8 hf[24];
    u32x4 yv[4][2];
#pragma unroll
    for (int kb = 0; kb < 24; ++kb) hf[kb] = __builtin_bit_cast(bf16x8, ld16(rs, WS_H + (unsigned)((tb * 4 + w) * 24 + kb) * 1024 + lane * 16, 0));
#pragma unroll
    for (int h = 0; h < 2; ++h)
#pragma unroll
      for (int p = 0; p < 4; ++p) yv[p][h] = ld16(rs, WS_Y + (unsigned)((p * 128 + tb * 4 + w) * 16 + 2 * G + h) * 1024 + lane * 16, 0);
    int at[4];
    {
      const int r = c >> 2, sg = (2 * (q & 1) + (r >> 1)) & 3;
#pragma unroll
      for (int T = 0; T < 4; ++T) at[T] = (4 * q + r) * 128 + ((T ^ sg) << 5) + (c & 3) * 8;
    }
    f32x4 oc[4] = {};
    bf16x8 wf[3][4];
    auto rdw = [&](auto KB) {
      constexpr int kb = KB.value, pt = kb / 3, pr = kb % 3, ro = (part_start(pt) + 2 * pr) * 2048;
      constexpr bool full = 2 * pr + 1 < part_cnt(pt);
#pragma unroll
      for (int T = 0; T < 4; ++T) {
        const bf16x4 lo = lds_tr(shm + at[T] + ro);
        const bf16x4 hi = full ? lds_tr(shm + at[T] + ro + 2048) : bf16x4{0, 0, 0, 0};
        wf[kb % 3][T] = cat(lo, hi);
      }
    };
    rdw(std::integral_constant<int, 0>{});
    rdw(std::integral_constant<int, 1>{});
    SCHED();
    static_for<0, 24>([&](auto KB) {
      constexpr int kb = KB.value;
      if constexpr (kb + 2 < 24) rdw(std::integral_constant<int, kb + 2>{});
#pragma unroll
      for (int T = 0; T < 4; ++T) oc[T] = MFMA(wf[kb % 3][T], hf[kb], oc[T]);
#pragma unroll
      for (int T = 0; T < 4; ++T) { __builtin_amdgcn_sched_group_barrier(0x8, 1, 0); __builtin_amdgcn_sched_group_barrier(0x100, 2, 0); }
      SCHED();
    });
    TSTAMP(13);
#pragma unroll
    for (int h = 0; h < 2; ++h) {
      float o[8];
#pragma unroll
      for (int r = 0; r < 4; ++r) {
        unsigned a = __float_as_uint(oc[2 * h][r]), b = __float_as_uint(oc[2 * h + 1][r]);
        swap16(a, b);
        o[r] = __uint_as_float(a); o[4 + r] = __uint_as_float(b);
      }
      unsigned res[4];
#pragma unroll
      for (int d = 0; d < 4; ++d) {
        const float yl = (bflo(yv[0][h][d]) + bflo(yv[1][h][d])) - (bflo(yv[2][h][d]) + bflo(yv[3][h][d]));
        const float yh = (bfhi(yv[0][h][d]) + bfhi(yv[1][h][d])) - (bfhi(yv[2][h][d]) + bfhi(yv[3][h][d]));
        res[d] = pack2(bflo(xv[h][d]) + yl * silu(o[2 * d]), bfhi(xv[h][d]) + yh * silu(o[2 * d + 1]));
      }
      *(u32x4*)(out + eo + h * 64) = u32x4{res[0], res[1], res[2], res[3]};
    }
  }
}

__global__ __launch_bounds__(512, 2) void kF(const char* __restrict__ x, const char* __restrict__ wsre, const char* __restrict__ wsim,
                                             const char* __restrict__ wdre, const char* __restrict__ wdim, const char* __restrict__ win,
                                             const char* __restrict__ wsp, const char* __restrict__ bsp, const char* __restrict__ wout,
                                             char* __restrict__ ws, char* __restrict__ out) {
  extern __shared__ __attribute__((aligned(1024))) char shm[];
  unsigned long long t0_;
  asm volatile("s_memrealtime %0" : "=s"(t0_));
  asm volatile("" :: "s"(x), "s"(wsre), "s"(wsim), "s"(wdre), "s"(wdim), "s"(win), "s"(wsp), "s"(bsp), "s"(wout), "s"(ws), "s"(out));
  asm volatile("s_waitcnt lgkmcnt(0)" : "+s"(t0_));
  TSTAMP(0);
  if (__builtin_amdgcn_readfirstlane(threadIdx.x >> 6) == 1) { unsigned long long* tp_ = (unsigned long long*)(ws + WS_DBG) + blockIdx.x * 16 + 14; asm volatile("global_store_dwordx2 %0, %1, off" :: "v"(tp_), "v"(t0_) : "memory"); }
  const int xcd = blockIdx.x & 7;
  if (xcd < 4) body<true>(x, wdre, wsp, win, bsp, wout, ws, out, shm);
  else body<false>(x, wdim, (xcd & 2) ? wsim : wsre, win, bsp, wout, ws, out, shm);
  TSTAMP(1);
}

__global__ void k0(char* ws) { if (threadIdx.x == 0) ((unsigned long long*)(ws + WS_DBG))[4096] = __builtin_amdgcn_s_memrealtime(); }
__global__ void kT(char* ws) {
  const unsigned long long tnow = __builtin_amdgcn_s_memrealtime();
  const unsigned long long* ts = (const unsigned long long*)(ws + WS_DBG);
  const int lane = threadIdx.x;
  unsigned long long lo = ~0ull, top = 0, lo14 = ~0ull; long long sd = 0;
  for (int i = lane; i < 256; i += 64) { unsigned long long a = ts[i * 16 + 1]; top = a > top ? a : top; a = ts[i * 16]; lo = a < lo ? a : lo; sd += (long long)(a - ts[i * 16 + 14]); a = ts[i * 16 + 14]; lo14 = a < lo14 ? a : lo14; }
  for (int o = 32; o; o >>= 1) { unsigned long long a = __shfl_xor(top, o); top = a > top ? a : top; a = __shfl_xor(lo, o); lo = a < lo ? a : lo; a = __shfl_xor(lo14, o); lo14 = a < lo14 ? a : lo14; sd += __shfl_xor(sd, o); }
  const unsigned long long pre = ts[4096];
  double d = 0.0;
  if (!(d > 0)) d = 0; if (d > 10000) d = 10000;
  const unsigned long long t0 = __builtin_amdgcn_s_memrealtime(), dt = (unsigned long long)(d * 10);
  for (unsigned it = 0; it < 2000000; ++it) { if (__builtin_amdgcn_s_memrealtime() - t0 >= dt) break; __builtin_amdgcn_s_sleep(1); }
}

extern "C" void kernel_launch(void* const* d_in, const int* in_sizes, int n_in, void* d_out, int out_size, void* d_ws, size_t ws_size,
                              hipStream_t stream) {
  (void)hipFuncSetAttribute((const void*)kF, hipFuncAttributeMaxDynamicSharedMemorySize, LDS_BYTES);
  kF<<<256, 512, LDS_BYTES, stream>>>((const char*)d_in[0], (const char*)d_in[1], (const char*)d_in[2], (const char*)d_in[3],
                                      (const char*)d_in[4], (const char*)d_in[6], (const char*)d_in[7], (const char*)d_in[8],
                                      (const char*)d_in[9], (char*)d_ws, (char*)d_out);
  kT<<<1, 64, 0, stream>>>((char*)d_ws);
}
```

```cpp
#include <hip/hip_runtime.h>
#include <hip/hip_bf16.h>
#include <stdint.h>
#include <type_traits>

typedef short bf16x8 __attribute__((ext_vector_type(8)));
typedef short bf16x4 __attribute__((ext_vector_type(4)));
typedef short v4i16_t __attribute__((ext_vector_type(4)));
typedef float f32x4 __attribute__((ext_vector_type(4)));
typedef float f32x2 __attribute__((ext_vector_type(2)));
typedef unsigned u32x4 __attribute__((ext_vector_type(4)));
typedef unsigned u32x2 __attribute__((ext_vector_type(2)));
typedef __bf16 bf16v2 __attribute__((ext_vector_type(2)));
typedef const __attribute__((address_space(1))) void* gptr_t;
typedef __attribute__((address_space(3))) void* lptr_t;
typedef __amdgpu_buffer_rsrc_t rsrc_t;

#define WAIT_V(n) asm volatile("s_waitcnt vmcnt(%0)" ::"n"(n) : "memory")
#define WAIT_L0() asm volatile("s_waitcnt lgkmcnt(0)" ::: "memory")
#define BAR() do { asm volatile("" ::: "memory"); __builtin_amdgcn_s_barrier(); asm volatile("" ::: "memory"); } while (0)
#define SCHED() __builtin_amdgcn_sched_barrier(0)
#define MFMA(a, b, c) __builtin_amdgcn_mfma_f32_16x16x32_bf16(a, b, c, 0, 0, 0)

#define PG_A 7
#define PG_T0 0
#define PG_DT 1
#define PG_N 7
#define PG_BS 1
#define PY_A 0
#define PY_T0 1
#define PY_DT 2
#define PY_I0 0
#define PY_N 0
#define PF_A 1
#define PF_AUX 0
#define OUT_AUX -1
constexpr int S_ = 2048, D_ = 512, F_ = 1376, FH = 688, NFT = 43;
constexpr int NT_ALL = 112;
constexpr unsigned WS_BIMG = 0;
constexpr unsigned WS_Z1 = 8u << 20;
constexpr unsigned WS_H = 12u << 20;
constexpr unsigned WS_Y = 16u << 20;
constexpr unsigned WS_XST = 24u << 20;
constexpr unsigned WS_ZST = WS_XST + 65536;
constexpr unsigned WS_FLG = WS_ZST + 262144;
constexpr unsigned WS_SALT = WS_FLG + 131072;
constexpr unsigned WS_END = 25u << 20;
constexpr unsigned WS_DBG = 26u << 20;
constexpr int FL_AB = 0, FL_Z2 = 256, FL_Z1 = 384, FL_H = 512, FL_Y = 640;

template <int I, int N, class F>
__device__ __forceinline__ void static_for(F&& f) {
  if constexpr (I < N) { f(std::integral_constant<int, I>{}); static_for<I + 1, N>(f); }
}
__device__ __forceinline__ unsigned pack2(float a, float b) {
  return __builtin_bit_cast(unsigned, __builtin_convertvector(f32x2{a, b}, bf16v2));
}
__device__ __forceinline__ float bflo(unsigned u) { return __uint_as_float(u << 16); }
__device__ __forceinline__ float bfhi(unsigned u) { return __uint_as_float(u & 0xffff0000u); }
template <int AUX = 0>
__device__ __forceinline__ void glds16o(const char* ubase, unsigned off, char* l) {
  asm volatile("" : "+v"(off));
  __builtin_amdgcn_global_load_lds((gptr_t)(ubase + off), (lptr_t)l, 16, 0, AUX);
}
__device__ __forceinline__ void swap16(unsigned& a, unsigned& b) {
  auto r = __builtin_amdgcn_permlane16_swap(a, b, false, false);
  a = r[0]; b = r[1];
}
template <int CTRL>
__device__ __forceinline__ float dpp_add(float v) {
  return v + __int_as_float(__builtin_amdgcn_update_dpp(0, __float_as_int(v), CTRL, 0xf, 0xf, true));
}
__device__ __forceinline__ float row16_sum(float v) {
  v = dpp_add<0x128>(v); v = dpp_add<0x124>(v); v = dpp_add<0x122>(v); v = dpp_add<0x121>(v);
  return v;
}
__device__ __forceinline__ float wave_sum(float v) {
  v = row16_sum(v);
  const int i = __float_as_int(v);
  return __int_as_float(__builtin_amdgcn_readlane(i, 0)) + __int_as_float(__builtin_amdgcn_readlane(i, 16)) +
         __int_as_float(__builtin_amdgcn_readlane(i, 32)) + __int_as_float(__builtin_amdgcn_readlane(i, 48));
}
__device__ __forceinline__ float gelu_tanh(float v) {
  const float c0 = -2.3022081986f, c1 = -0.1029432397f;
  const float e = __builtin_amdgcn_exp2f(v * __builtin_fmaf(v * v, c1, c0));
  return v * __builtin_amdgcn_rcpf(1.0f + e);
}
__device__ __forceinline__ float silu(float v) {
  const float e = __builtin_amdgcn_exp2f(v * -1.4426950409f);
  return v * __builtin_amdgcn_rcpf(1.0f + e);
}
__device__ __forceinline__ bf16x8 lds_frag(const char* p) { return *(const bf16x8*)p; }
__device__ __forceinline__ bf16x4 lds_tr(const char* p) {
  return __builtin_bit_cast(bf16x4, __builtin_amdgcn_ds_read_tr16_b64_v4i16((__attribute__((address_space(3))) v4i16_t*)p));
}
__device__ __forceinline__ bf16x8 cat(bf16x4 a, bf16x4 b) { return __builtin_shufflevector(a, b, 0, 1, 2, 3, 4, 5, 6, 7); }

constexpr int SC1 = 16;
__device__ __forceinline__ void st16(rsrc_t rs, unsigned voff, unsigned soff, u32x4 v) { __builtin_amdgcn_raw_buffer_store_b128(v, rs, voff, soff, SC1); }
__device__ __forceinline__ u32x4 ld16(rsrc_t rs, unsigned voff, unsigned soff) { return __builtin_amdgcn_raw_buffer_load_b128(rs, voff, soff, SC1); }
__device__ __forceinline__ void granule_store(rsrc_t rs, unsigned off, float v0, float v1, u32x2 tag) {
  st16(rs, off, 0, u32x4{__float_as_uint(v0), tag[0], __float_as_uint(v1), tag[1]});
}
constexpr unsigned SPIN_MAX = 1u << 18;
__device__ __forceinline__ void flag_set(rsrc_t rs, int idx, u32x2 tag, int lane) {
  if (lane == 0) __builtin_amdgcn_raw_buffer_store_b64(tag, rs, WS_FLG + idx * 128, 0, SC1);
}
__device__ __forceinline__ void flag_wait(rsrc_t rs, int idx, u32x2 tag) {
  for (unsigned spin = 0; spin < SPIN_MAX; ++spin) {
    const u32x2 g = __builtin_amdgcn_raw_buffer_load_b64(rs, WS_FLG + idx * 128, 0, SC1);
    if (__all((g[0] == tag[0]) & (g[1] == tag[1]))) break;
    __builtin_amdgcn_s_sleep(1);
  }
  asm volatile("" ::: "memory");
}

#define TSTAMPC(slot) do {} while (0)
#define TSTAMP(slot) do {} while (0)
#define TSTAMPW(slot, wv) do {} while (0)

__device__ __forceinline__ constexpr int part_start(int p) { return p < 3 ? 6 * p : 18 + 5 * (p - 3); }
__device__ __forceinline__ constexpr int part_cnt(int p) { return p < 3 ? 6 : 5; }

constexpr int A_STAGE = 36864, A_NS = 4, A_XO = 0, A_DO = 16384, A_WO = 24576;
constexpr int L_HO = 69632;
constexpr int L_CS = A_NS * A_STAGE;
constexpr int L_XRS = L_CS + 2048;
constexpr int L_XNM = L_XRS + 512;
constexpr int L_ZPS = L_XNM + 512;
constexpr int L_ZPQ = L_ZPS + 1024;
constexpr int L_ZRS = L_ZPQ + 1024;
constexpr int L_ZNM = L_ZRS + 512;
constexpr int L_DUM = L_ZNM + 512;
constexpr int LDS_BYTES = L_DUM + 1024;

template <bool G>
struct PB {
  static constexpr int NA = G ? 4 : 8, NB = G ? 3 : 4, NP = NA + NB;
  static constexpr int NS = G ? 5 : 3, NSTG = G ? 32 : 16, PF = G ? 4 : 2;
  static constexpr int ABYTES = NA * 4096, BBYTES = NB * 4096, BOFF = G ? 81920 : 98304;
  static constexpr int MT = G ? 2 : 8, NT = G ? 6 : 4, BKS = BBYTES / 2;
  static constexpr int cnt(int s) { return s >= NSTG ? 0 : (s < PF ? NB : NP); }
  static constexpr int span(int s0, int s1) { return s0 > s1 ? 0 : cnt(s0) + span(s0 + 1, s1); }
};

template <bool Z2>
__device__ __forceinline__ void body(const char* __restrict__ x, const char* __restrict__ wd, const char* __restrict__ wsq,
                                     const char* __restrict__ win, const char* __restrict__ bsp, const char* __restrict__ wout,
                                     char* __restrict__ ws, char* __restrict__ out, char* shm) {
  typedef PB<Z2> P;
  const int tid = threadIdx.x, w = __builtin_amdgcn_readfirstlane(tid >> 6), lane = tid & 63;
  const int c = lane & 15, q = lane >> 4;
  const int xcd = blockIdx.x & 7, bi = blockIdx.x >> 3;
  const bool prod = w >= 4;
  const int pw = w & 3, wr = pw >> 1, wc = pw & 1;
  const int panel = (xcd & 1) * 8 + (bi >> 2), j = (xcd >> 1) * 4 + (bi & 3);
  const int part = j & 7, fstart = part_start(part), fcnt = part_cnt(part);
  const int gpanel = xcd * 4 + (bi >> 3), gpart = bi & 7, gstart = part_start(gpart), gcnt = part_cnt(gpart);
  const int ymat = (xcd >> 1) & 1, ykh = xcd & 1, yp = bi >> 2, yeg = bi & 3;
  const int tb = xcd * 4 + (bi >> 3), G = bi & 7;

  const rsrc_t rs = __builtin_amdgcn_make_buffer_rsrc((void*)ws, 0, WS_END, 0x00020000);
  u32x2 tag = {0, 0};
  f32x4 ac[2][5];
  unsigned oa[P::NA], ob[P::NB];
  const char* Ab = wsq + (Z2 ? (size_t)gpanel * 128 * 4096 : (size_t)yp * 256 * 4096 + ykh * 2048);
  const char* Bb = ws + WS_BIMG + (Z2 ? 0 : (size_t)ykh * 32 * (NT_ALL * 1024));
  auto issueBA = [&](int t, int slot) {
    const char* ab = Ab + t * 128;
    asm volatile("" : "+s"(ab));
#pragma unroll
    for (int i = 0; i < P::NA; ++i) glds16o(ab, oa[i], shm + slot * P::ABYTES + (pw * P::NA + i) * 1024);
  };
  auto issueBB = [&](int t, int slot) {
    const char* bb = Bb + (size_t)t * (2 * NT_ALL * 1024);
    asm volatile("" : "+s"(bb));
#pragma unroll
    for (int i = 0; i < P::NB; ++i) glds16o<SC1>(bb, ob[i], shm + P::BOFF + slot * P::BBYTES + (pw * P::NB + i) * 1024);
  };

  auto touch = [&](const char* ubase, unsigned off) {
    asm volatile("" : "+v"(off));
    __builtin_amdgcn_global_load_lds((gptr_t)(ubase + off), (lptr_t)(shm + L_DUM + pw * 256), 4, 0, PF_AUX);
  };
  const unsigned tw0 = Z2 ? (unsigned)(gpanel * 128 + gpart * 16 + (lane >> 2)) * 4096u + (lane & 3) * 128
                          : (unsigned)(yp * 256 + yeg * 64 + (lane >> 2)) * 4096u + ykh * 2048 + (lane & 3) * 128;
  auto touchW = [&](int i) { touch(wsq, tw0 + (Z2 ? (unsigned)i * 512u : (unsigned)(i >> 2) * 512u + (unsigned)(i & 3) * 65536u)); };
  constexpr int TW_A = Z2 ? PG_A : PY_A, TW_T0 = Z2 ? PG_T0 : PY_T0, TW_DT = Z2 ? PG_DT : PY_DT, TW_I0 = Z2 ? 1 : PY_I0, TW_N = Z2 ? PG_N + 1 : PY_N;

  float* csum = (float*)(shm + L_CS);
  float* xrs = (float*)(shm + L_XRS);
  float* xnm = (float*)(shm + L_XNM);
  if (prod) {
    unsigned ox[4], od[2], ow[3];
#pragma unroll
    for (int i = 0; i < 4; ++i) {
      const int row = 8 * (pw * 4 + i) + (lane >> 3);
      ox[i] = (unsigned)(panel * 128 + row) * 1024u + (unsigned)(((lane & 7) ^ ((row >> 1) & 7)) << 4);
    }
#pragma unroll
    for (int i = 0; i < 2; ++i) {
      const int row = 8 * (pw * 2 + i) + (lane >> 3);
      od[i] = (unsigned)((j & 7) * 64 + row) * 1024u + (unsigned)(((lane & 7) ^ ((row >> 1) & 7)) << 4);
    }
#pragma unroll
    for (int i = 0; i < 3; ++i) {
      const int o = (pw * 3 + i) * 1024 + lane * 16, row = o / 192, ch = (o % 192) >> 4;
      const int u = (ch >> 1) ^ ((row >> 3) & 1);
      const int ft = fstart + (u < fcnt ? u : fcnt - 1);
      ow[i] = (unsigned)row * 2752u + (unsigned)((Z2 ? FH * 2 : 0) + ft * 32 + (ch & 1) * 16);
    }
    u32x2 salt;
    {
      const char* ps = ws + WS_SALT;
      asm volatile("global_load_dwordx2 %0, %1, off sc1" : "=&v"(salt) : "v"(ps) : "memory");
    }
    auto issue = [&](int t) {
      char* st = shm + (t % A_NS) * A_STAGE;
      const char* xb = x + t * 128;
      const char* db = wd + t * 128;
      const char* wb = win + (size_t)t * (64 * 2752);
      asm volatile("" : "+s"(xb), "+s"(db), "+s"(wb));
#pragma unroll
      for (int i = 0; i < 4; ++i) glds16o(xb, ox[i], st + A_XO + (pw * 4 + i) * 1024);
#pragma unroll
      for (int i = 0; i < 2; ++i) glds16o(db, od[i], st + A_DO + (pw * 2 + i) * 1024);
#pragma unroll
      for (int i = 0; i < 3; ++i) glds16o(wb, ow[i], st + A_WO + (pw * 3 + i) * 1024);
    };
#pragma unroll
    for (int t = 0; t < A_NS; ++t) issue(t);
#pragma unroll
    for (int i = 0; i < P::NA; ++i) {
      const int row = (pw * P::NA + i) * 8 + (lane >> 3);
      oa[i] = (unsigned)row * 4096u + (unsigned)(((lane & 7) ^ ((row >> 1) & 7)) << 4);
    }
#pragma unroll
    for (int i = 0; i < P::NB; ++i) {
      const int n = (pw & 1) * P::NB + i;
      const int nt = Z2 ? 64 + gstart + (n < gcnt ? n : gcnt - 1) : ymat * 32 + yeg * 8 + n;
      ob[i] = (unsigned)(((pw >> 1) * NT_ALL + nt) * 1024 + lane * 16);
    }
    asm volatile("s_waitcnt vmcnt(27)" : "+v"(salt)::"memory");
    BAR();
    {
      const unsigned long long tag64 = (((unsigned long long)salt[1] << 32) | salt[0]) + 0x9E3779B97F4A7C15ull;
      tag = u32x2{(unsigned)tag64, (unsigned)(tag64 >> 32)};
    }
    const unsigned gxo = WS_XST + (panel * 128 + pw * 32 + (lane & 31)) * 16;
    u32x4 gx;
    static_for<0, 7>([&](auto T) {
      constexpr int t = T.value;
      if constexpr (t < 6) WAIT_V(t <= 4 ? 18 : 9);
      else asm volatile("s_waitcnt vmcnt(%1)" : "+v"(gx) : "n"(P::PF * P::NA) : "memory");
      BAR();
      if constexpr (t + A_NS < 8) issue(t + A_NS);
      if constexpr (t == 5) {
        const char* pg = ws + gxo;
        asm volatile("global_load_dwordx4 %0, %1, off sc1" : "=&v"(gx) : "v"(pg) : "memory");
#pragma unroll
        for (int s = 0; s < P::PF; ++s) issueBA(s, s);
      }
    });
    if (!__all((gx[1] == tag[0]) & (gx[3] == tag[1]))) {
      for (unsigned spin = 0; spin < SPIN_MAX; ++spin) {
        gx = ld16(rs, gxo, 0);
        if (__all((gx[1] == tag[0]) & (gx[3] == tag[1]))) break;
        __builtin_amdgcn_s_sleep(1);
      }
    }
    xrs[pw * 32 + (lane & 31)] = __uint_as_float(gx[0]);
    xnm[pw * 32 + (lane & 31)] = __uint_as_float(gx[2]);
    WAIT_L0();
  } else {
    u32x2 salt; u32x4 xr0, xr1;
    if (w == 1) touch(wd, (unsigned)(((blockIdx.x >> 3) * 4 + (xcd & 3)) * 4) * 1024u + lane * 64);
    if (w == 2) touch(win, (unsigned)(256 + blockIdx.x) * 2752u + (lane < 43 ? lane : 42) * 64);
    {
      const char* ps = ws + WS_SALT;
      const char* pr = x + (size_t)(panel * 128 + 8 * j + 2 * w) * 1024 + lane * 16;
      asm volatile("global_load_dwordx2 %0, %1, off sc1" : "=&v"(salt) : "v"(ps) : "memory");
      asm volatile("global_load_dwordx4 %0, %1, off" : "=&v"(xr0) : "v"(pr) : "memory");
      asm volatile("global_load_dwordx4 %0, %1, off offset:1024" : "=&v"(xr1) : "v"(pr) : "memory");
    }
    f32x4 acc[4][5] = {};
    f32x4 cs[5] = {};
    bf16x8 fx[2][4], fd[2][2], fw[2][3];
    const bf16x8 ones = {0x3F80, 0x3F80, 0x3F80, 0x3F80, 0x3F80, 0x3F80, 0x3F80, 0x3F80};
    int ax[2], ad[2], aw[3];
#pragma unroll
    for (int ks = 0; ks < 2; ++ks) {
      const int sw = ((ks * 4 + q) ^ ((c >> 1) & 7)) << 4;
      ax[ks] = A_XO + (64 * wr + c) * 128 + sw;
      ad[ks] = A_DO + (32 * wc + c) * 128 + sw;
    }
#pragma unroll
    for (int i = 0; i < 3; ++i) aw[i] = A_WO + (8 * q + (c >> 2)) * 192 + (((3 * wc + i) ^ (q & 1)) << 5) + (c & 3) * 8;
    asm volatile("s_waitcnt vmcnt(0)" : "+v"(salt), "+v"(xr0), "+v"(xr1)::"memory");
    {
      const unsigned long long tag64 = (((unsigned long long)salt[1] << 32) | salt[0]) + 0x9E3779B97F4A7C15ull;
      tag = u32x2{(unsigned)tag64, (unsigned)(tag64 >> 32)};
      float v[2][8], mean[2], ss[2];
#pragma unroll
      for (int i = 0; i < 4; ++i) { v[0][2 * i] = bflo(xr0[i]); v[0][2 * i + 1] = bfhi(xr0[i]); v[1][2 * i] = bflo(xr1[i]); v[1][2 * i + 1] = bfhi(xr1[i]); }
#pragma unroll
      for (int r = 0; r < 2; ++r)
        mean[r] = wave_sum(((v[r][0] + v[r][1]) + (v[r][2] + v[r][3])) + ((v[r][4] + v[r][5]) + (v[r][6] + v[r][7]))) * (1.0f / D_);
#pragma unroll
      for (int r = 0; r < 2; ++r) {
        ss[r] = 0.f;
#pragma unroll
        for (int i = 0; i < 8; ++i) { const float d = v[r][i] - mean[r]; ss[r] = __builtin_fmaf(d, d, ss[r]); }
        ss[r] = __builtin_amdgcn_rsqf(wave_sum(ss[r]) * (1.0f / D_) + 1e-5f);
      }
      const int r = lane & 1;
      const float rsv = r ? ss[1] : ss[0], mv = r ? mean[1] : mean[0];
      if (lane < 2) granule_store(rs, WS_XST + (panel * 128 + 8 * j + 2 * w + r) * 16, rsv, -mv * rsv, tag);
    }
    auto rd = [&](int set, int t, int ks) {
      const char* st = shm + (t % A_NS) * A_STAGE;
#pragma unroll
      for (int i = 0; i < 2; ++i) fd[set][i] = lds_frag(st + ad[ks] + i * 2048);
#pragma unroll
      for (int m = 0; m < 4; ++m) fx[set][m] = lds_frag(st + ax[ks] + m * 2048);
#pragma unroll
      for (int i = 0; i < 3; ++i) fw[set][i] = cat(lds_tr(st + aw[i] + ks * 6144), lds_tr(st + aw[i] + ks * 6144 + 768));
    };
    auto mm = [&](int set) {
#pragma unroll
      for (int i = 0; i < 2; ++i)
#pragma unroll
        for (int m = 0; m < 4; ++m) acc[m][i] = MFMA(fx[set][m], fd[set][i], acc[m][i]);
#pragma unroll
      for (int i = 0; i < 3; ++i)
#pragma unroll
        for (int m = 0; m < 4; ++m)
          acc[m][2 + i] = Z2 ? MFMA(fx[set][m], fw[set][i], acc[m][2 + i]) : MFMA(fw[set][i], fx[set][m], acc[m][2 + i]);
    };
    auto mmcs = [&](int set) {
#pragma unroll
      for (int i = 0; i < 2; ++i) cs[i] = MFMA(ones, fd[set][i], cs[i]);
#pragma unroll
      for (int i = 0; i < 3; ++i) cs[2 + i] = Z2 ? MFMA(ones, fw[set][i], cs[2 + i]) : MFMA(fw[set][i], ones, cs[2 + i]);
    };
    auto interleave = [&]() {
#pragma unroll
      for (int i = 0; i < 12; ++i) { __builtin_amdgcn_sched_group_barrier(0x8, 1, 0); __builtin_amdgcn_sched_group_barrier(0x100, 1, 0); }
      __builtin_amdgcn_sched_group_barrier(0x8, 8, 0);
    };
    BAR();
    TSTAMP(2);
    rd(0, 0, 0);
    WAIT_L0(); SCHED();
    static_for<0, 8>([&](auto T) {
      constexpr int t = T.value;
      if constexpr (t >= TW_T0 && (t - TW_T0) % TW_DT == 0 && (t - TW_T0) / TW_DT < TW_A) { if (w == 1) touchW(TW_I0 + (t - TW_T0) / TW_DT); }
      rd(1, t, 1); mm(0); interleave(); SCHED();
      if (wr == 0) mmcs(0);
      if constexpr (t < 7) {
        WAIT_L0(); BAR(); SCHED();
        rd(0, t + 1, 0); mm(1); interleave(); SCHED();
      } else {
        mm(1);
      }
      if (wr == 1) mmcs(1);
    });
    TSTAMP(3);
#pragma unroll
    for (int m = 0; m < 2; ++m)
#pragma unroll
      for (int i = 0; i < 5; ++i) {
        *(f32x4*)(shm + L_HO + (pw * 10 + m * 5 + i) * 1024 + lane * 16) = acc[2 + m][i];
        ac[m][i] = acc[m][i];
      }
#pragma unroll
    for (int i = 0; i < 5; ++i) {
      if (i < 2 || Z2) { if (q == 0) csum[((wc * 5 + i) * 16 + c) * 2 + wr] = cs[i][0]; }
      else if (c == 0) {
#pragma unroll
        for (int r = 0; r < 4; ++r) csum[((wc * 5 + i) * 16 + 4 * q + r) * 2 + wr] = cs[i][r];
      }
    }
    WAIT_L0();
  }
  BAR();
  TSTAMP(4);

  {
    const int wr4 = 2 * wr + (w >> 2);
    if (prod) {
#pragma unroll
      for (int m = 0; m < 2; ++m)
#pragma unroll
        for (int i = 0; i < 5; ++i) ac[m][i] = *(const f32x4*)(shm + L_HO + (pw * 10 + m * 5 + i) * 1024 + lane * 16);
    }
    const unsigned bimg = WS_BIMG + (unsigned)(panel * 4 + wr4) * (NT_ALL * 1024) + (c + 16 * (((q & 1) << 1) | (q >> 1))) * 16;
    f32x4 rs4[2], nm4[2];
#pragma unroll
    for (int m = 0; m < 2; ++m) {
      rs4[m] = *(const f32x4*)(xrs + 32 * wr4 + 16 * m + 4 * q);
      nm4[m] = *(const f32x4*)(xnm + 32 * wr4 + 16 * m + 4 * q);
    }
    auto colsum1 = [&](int i) { const f32x2 t = *(const f32x2*)(csum + ((wc * 5 + i) * 16 + c) * 2); return t[0] + t[1]; };
    auto store_bt = [&](const f32x4& v0, const f32x4& v1, int ntile) {
      unsigned a0 = pack2(v0[0], v0[1]), a1 = pack2(v0[2], v0[3]), b0 = pack2(v1[0], v1[1]), b1 = pack2(v1[2], v1[3]);
      swap16(a0, b0); swap16(a1, b1);
      st16(rs, bimg + ntile * 1024, 0, u32x4{a0, a1, b0, b1});
    };
#pragma unroll
    for (int i = 0; i < 2; ++i) {
      const float cv = colsum1(i);
      store_bt(rs4[0] * ac[0][i] + nm4[0] * cv, rs4[1] * ac[1][i] + nm4[1] * cv, (j >> 3) * 32 + (j & 7) * 4 + 2 * wc + i);
    }

    if constexpr (!Z2) {
      float rl[2], nl[2];
#pragma unroll
      for (int m = 0; m < 2; ++m) { rl[m] = xrs[32 * wr4 + 16 * m + c]; nl[m] = xnm[32 * wr4 + 16 * m + c]; }
      u32x4 zp[3];
#pragma unroll
      for (int i = 0; i < 3; ++i) {
        f32x4 cv;
#pragma unroll
        for (int r = 0; r < 4; ++r) { const f32x2 t = *(const f32x2*)(csum + ((wc * 5 + 2 + i) * 16 + 4 * q + r) * 2); cv[r] = t[0] + t[1]; }
#pragma unroll
        for (int m = 0; m < 2; ++m) {
          const f32x4 v = rl[m] * ac[m][2 + i] + nl[m] * cv;
          zp[i][2 * m] = pack2(gelu_tanh(v[0]), gelu_tanh(v[1]));
          zp[i][2 * m + 1] = pack2(gelu_tanh(v[2]), gelu_tanh(v[3]));
        }
      }
      asm volatile("" : "+v"(zp[0]), "+v"(zp[1]), "+v"(zp[2]));
      WAIT_V(0); BAR();
      TSTAMP(5);
      if (w == 0) flag_set(rs, FL_AB + panel * 16 + j, tag, lane);
      const unsigned z1o = WS_Z1 + (unsigned)(panel * 4 + wr4) * (NFT * 1024) + lane * 16;
#pragma unroll
      for (int i = 0; i < 3; ++i)
        if (3 * wc + i < fcnt) st16(rs, z1o + (fstart + 3 * wc + i) * 1024, 0, zp[i]);
      if (w == 0) flag_wait(rs, FL_AB + (ykh * 8 + ((lane >> 1) & 7)) * 16 + ymat * 8 + 2 * yeg + (lane & 1), tag);
      WAIT_V(0); BAR();
      TSTAMP(8);
      if (w == 1) flag_set(rs, FL_Z1 + panel * 8 + part, tag, lane);
    } else {
      f32x4 zv[2][3];
      f32x4 ssum[2] = {}, ssq[2] = {};
#pragma unroll
      for (int i = 0; i < 3; ++i) {
        const float cv = colsum1(2 + i);
        const float valid = (3 * wc + i < fcnt) ? 1.0f : 0.0f;
#pragma unroll
        for (int m = 0; m < 2; ++m) {
          const f32x4 v = rs4[m] * ac[m][2 + i] + nm4[m] * cv;
#pragma unroll
          for (int r = 0; r < 4; ++r) zv[m][i][r] = gelu_tanh(v[r]) * valid;
          ssum[m] += zv[m][i];
          ssq[m] += zv[m][i] * zv[m][i];
        }
      }
      float* zps = (float*)(shm + L_ZPS);
      float* zpq = (float*)(shm + L_ZPQ);
      float* zrs = (float*)(shm + L_ZRS);
      float* znm = (float*)(shm + L_ZNM);
#pragma unroll
      for (int m = 0; m < 2; ++m) {
#pragma unroll
        for (int r = 0; r < 4; ++r) { ssum[m][r] = row16_sum(ssum[m][r]); ssq[m][r] = row16_sum(ssq[m][r]); }
        if (c == 0) {
          *(f32x4*)(zps + wc * 128 + 32 * wr4 + 16 * m + 4 * q) = ssum[m];
          *(f32x4*)(zpq + wc * 128 + 32 * wr4 + 16 * m + 4 * q) = ssq[m];
        }
      }
      WAIT_V(0); WAIT_L0(); BAR();
      TSTAMP(5);
      if (w == 2) flag_set(rs, FL_AB + panel * 16 + j, tag, lane);
      if (w < 2) {
        const unsigned zoff = WS_ZST + (panel * 8 * 128 + tid) * 16;
        granule_store(rs, zoff + part * 2048, zps[tid] + zps[128 + tid], zpq[tid] + zpq[128 + tid], tag);
        for (unsigned spin = 0; spin < SPIN_MAX; ++spin) {
          bool ok = true; float s = 0.f, sq = 0.f;
#pragma unroll
          for (int p = 0; p < 8; ++p) {
            const u32x4 g = ld16(rs, zoff + p * 2048, 0);
            ok = ok & (g[1] == tag[0]) & (g[3] == tag[1]);
            s += __uint_as_float(g[0]); sq += __uint_as_float(g[2]);
          }
          const float mean = s * (1.0f / FH);
          const float var = __builtin_fmaxf(sq * (1.0f / FH) - mean * mean, 0.0f);
          const float rsv = __builtin_amdgcn_rsqf(var + 1e-5f);
          zrs[tid] = rsv; znm[tid] = -mean * rsv;
          if (__all(ok)) break;
          __builtin_amdgcn_s_sleep(1);
        }
      }
      WAIT_L0(); BAR();
      TSTAMP(6);
      f32x4 zr[2], zn[2];
#pragma unroll
      for (int m = 0; m < 2; ++m) {
        zr[m] = *(const f32x4*)(zrs + 32 * wr4 + 16 * m + 4 * q);
        zn[m] = *(const f32x4*)(znm + 32 * wr4 + 16 * m + 4 * q);
      }
#pragma unroll
      for (int i = 0; i < 3; ++i)
        if (3 * wc + i < fcnt) store_bt(zv[0][i] * zr[0] + zn[0], zv[1][i] * zr[1] + zn[1], 64 + fstart + 3 * wc + i);
      WAIT_V(0); BAR();
      TSTAMP(7);
      if (w == 0) {
        flag_set(rs, FL_Z2 + panel * 8 + part, tag, lane);
        flag_wait(rs, lane < 16 ? FL_Z2 + lane * 8 + gpart : FL_Z1 + gpanel * 8 + gpart, tag);
      }
      BAR();
      TSTAMP(8);
    }
  }

  if (prod) {
#pragma unroll
    for (int s = 0; s < P::PF; ++s) issueBB(s, s);
#pragma unroll
    for (int s = P::PF; s < P::NS; ++s) { issueBA(s, s); issueBB(s, s); }
    WAIT_V(P::span(1, P::NS - 1)); BAR();
    constexpr int NPEEL = P::PF > 2 ? P::PF - 2 : 0;
    static_for<0, NPEEL>([&](auto T) {
      constexpr int t = T.value;
      WAIT_V(P::span(t + 2, t + P::NS - 1)); BAR();
      issueBA(t + P::NS, t % P::NS); issueBB(t + P::NS, t % P::NS);
    });
    int slot = NPEEL % P::NS;
    for (int t = NPEEL; t < P::NSTG - P::NS; ++t) {
      WAIT_V((P::NS - 2) * P::NP); BAR();
      issueBA(t + P::NS, slot); issueBB(t + P::NS, slot);
      slot = (slot + 1 == P::NS) ? 0 : slot + 1;
    }
    static_for<0, P::NS>([&](auto I) {
      constexpr int rem = P::NS - 2 - I.value;
      WAIT_V((rem > 0 ? rem : 0) * P::NP); BAR();
    });
    const int rr = lane >> 3;
    const unsigned lo = (unsigned)rr * 1024u + (unsigned)(G * 128 + (((lane & 7) ^ (2 * ((rr >> 1) & 3))) << 4));
#pragma unroll
    for (int k = 0; k < 22; ++k) {
      if (k == 10) BAR();
      int Pc = 4 * k + pw; Pc = Pc < 85 ? Pc : 85;
      glds16o(wout + (size_t)Pc * 8192, lo, shm + Pc * 1024);
    }
    WAIT_V(0); BAR();
  } else {
    f32x4 acc[P::MT][P::NT] = {};
    u32x4 z1[6]; float bs[2];
    if constexpr (Z2) {
#pragma unroll
      for (int n = 0; n < 6; ++n)
        z1[n] = ld16(rs, WS_Z1 + (unsigned)((gpanel * 4 + w) * NFT + gstart + (n < gcnt ? n : gcnt - 1)) * 1024 + lane * 16, 0);
#pragma unroll
      for (int m = 0; m < 2; ++m) bs[m] = bflo(*(const unsigned short*)(bsp + (gpanel * 128 + w * 32 + m * 16 + c) * 2));
    }
    {
      constexpr int MT = P::MT, NT = P::NT;
      bf16x8 fa[2][MT], fb[2][NT];
      int aoff[2];
#pragma unroll
      for (int ks = 0; ks < 2; ++ks) aoff[ks] = ((Z2 ? 32 * w : 128 * wr) + c) * 128 + (((ks * 4 + q) ^ ((c >> 1) & 7)) << 4);
      const int boff = P::BOFF + (Z2 ? 0 : wc * 4096) + lane * 16;
      auto rd = [&](int set, int slot, int ks) {
        const char* sa = shm + slot * P::ABYTES;
        const char* sb = shm + slot * P::BBYTES;
#pragma unroll
        for (int i = 0; i < (MT > NT ? MT : NT); ++i) {
          if (i < NT) fb[set][i] = lds_frag(sb + boff + ks * P::BKS + i * 1024);
          if (i < MT) fa[set][i] = lds_frag(sa + aoff[ks] + i * 2048);
        }
      };
      auto mm = [&](int set) {
#pragma unroll
        for (int m = 0; m < MT; ++m)
#pragma unroll
          for (int n = 0; n < NT; ++n) acc[m][n] = MFMA(fb[set][n], fa[set][m], acc[m][n]);
      };
      auto interleave = [&]() {
#pragma unroll
        for (int i = 0; i < MT + NT; ++i) { __builtin_amdgcn_sched_group_barrier(0x8, 1, 0); __builtin_amdgcn_sched_group_barrier(0x100, 1, 0); }
        __builtin_amdgcn_sched_group_barrier(0x8, MT * NT - MT - NT, 0);
      };
      BAR();
      TSTAMP(9); TSTAMPC(14);
      rd(0, 0, 0);
      WAIT_L0(); SCHED();
      int slot = 0;
      for (int t = 0; t < P::NSTG; ++t) {
        if constexpr (Z2) { if (w == 1 && (t & ((1 << PG_BS) - 1)) == 0 && TW_I0 + TW_A + (t >> PG_BS) < TW_N) touchW(TW_I0 + TW_A + (t >> PG_BS)); }
        else { if (w == 1 && TW_I0 + TW_A + t < TW_N) touchW(TW_I0 + TW_A + t); }
        rd(1, slot, 1); mm(0); interleave(); SCHED();
        WAIT_L0(); BAR(); SCHED();
        slot = (slot + 1 == P::NS) ? 0 : slot + 1;
        rd(0, slot, 0);
        mm(1); interleave(); SCHED();
      }
    }
    TSTAMP(10); TSTAMPC(15);
    if constexpr (Z2) {
#pragma unroll
      for (int m = 0; m < 2; ++m)
#pragma unroll
        for (int p = 0; p < 3; ++p) {
          u32x4 o;
#pragma unroll
          for (int h = 0; h < 2; ++h) {
            const int n = 2 * p + h;
            const unsigned zl = z1[n][2 * m], zh = z1[n][2 * m + 1];
            const f32x4 g = acc[m][n] + bs[m];
            o[2 * h] = pack2(bflo(zl) * g[0], bfhi(zl) * g[1]);
            o[2 * h + 1] = pack2(bflo(zh) * g[2], bfhi(zh) * g[3]);
          }
          if (p == 2 && gcnt < 6) { o[2] = 0; o[3] = 0; }
          st16(rs, WS_H + (unsigned)((gpanel * 8 + 2 * w + m) * 24 + gpart * 3 + p) * 1024 + lane * 16, 0, o);
        }
    } else {
      const unsigned yo = WS_Y + (unsigned)(((ymat * 2 + ykh) * 128 + yp * 16 + wr * 8) * 16 + yeg * 4 + wc * 2) * 1024 + lane * 16;
#pragma unroll
      for (int m = 0; m < 8; ++m)
#pragma unroll
        for (int h = 0; h < 2; ++h) {
          const f32x4 v0 = acc[m][2 * h], v1 = acc[m][2 * h + 1];
          unsigned a0 = pack2(v0[0], v0[1]), a1 = pack2(v0[2], v0[3]), b0 = pack2(v1[0], v1[1]), b1 = pack2(v1[2], v1[3]);
          swap16(a0, b0); swap16(a1, b1);
          st16(rs, yo + m * 16384 + h * 1024, 0, u32x4{a0, a1, b0, b1});
        }
    }
    WAIT_V(0); BAR();
    TSTAMP(11);
    if (w == 0) flag_set(rs, Z2 ? FL_H + gpanel * 8 + gpart : FL_Y + ((ymat * 2 + ykh) * 8 + yp) * 4 + yeg, tag, lane);
    const size_t eo = (size_t)(tb * 64 + w * 16 + c) * 1024 + (G * 64 + 16 * (q & 1) + 8 * (q >> 1)) * 2;
    u32x4 xv[2];
#pragma unroll
    for (int h = 0; h < 2; ++h) xv[h] = *(const u32x4*)(x + eo + h * 64);
    if (w == 0) flag_wait(rs, lane < 8 ? FL_H + (tb >> 1) * 8 + lane : FL_Y + (((lane - 8) & 3) * 8 + (tb >> 2)) * 4 + (G >> 1), tag);
    if (w == 1 && blockIdx.x == 0) {
#pragma unroll
      for (int k = 0; k < 4; ++k) flag_wait(rs, FL_AB + k * 64 + lane, tag);
      if (lane == 0) __builtin_amdgcn_raw_buffer_store_b64(tag, rs, WS_SALT, 0, SC1);
    }
    BAR();
    TSTAMP(12);
    bf16x8 hf[24];
    u32x4 yv[4][2];
#pragma unroll
    for (int kb = 0; kb < 24; ++kb) hf[kb] = __builtin_bit_cast(bf16x8, ld16(rs, WS_H + (unsigned)((tb * 4 + w) * 24 + kb) * 1024 + lane * 16, 0));
#pragma unroll
    for (int h = 0; h < 2; ++h)
#pragma unroll
      for (int p = 0; p < 4; ++p) yv[p][h] = ld16(rs, WS_Y + (unsigned)((p * 128 + tb * 4 + w) * 16 + 2 * G + h) * 1024 + lane * 16, 0);
    int at[4];
    {
      const int r = c >> 2, sg = (2 * (q & 1) + (r >> 1)) & 3;
#pragma unroll
      for (int T = 0; T < 4; ++T) at[T] = (4 * q + r) * 128 + ((T ^ sg) << 5) + (c & 3) * 8;
    }
    f32x4 oc[4] = {};
    bf16x8 wf[3][4];
    auto rdw = [&](auto KB) {
      constexpr int kb = KB.value, pt = kb / 3, pr = kb % 3, ro = (part_start(pt) + 2 * pr) * 2048;
      constexpr bool full = 2 * pr + 1 < part_cnt(pt);
#pragma unroll
      for (int T = 0; T < 4; ++T) {
        const bf16x4 lo = lds_tr(shm + at[T] + ro);
        const bf16x4 hi = full ? lds_tr(shm + at[T] + ro + 2048) : bf16x4{0, 0, 0, 0};
        wf[kb % 3][T] = cat(lo, hi);
      }
    };
    rdw(std::integral_constant<int, 0>{});
    rdw(std::integral_constant<int, 1>{});
    SCHED();
    static_for<0, 24>([&](auto KB) {
      constexpr int kb = KB.value;
      if constexpr (kb + 2 < 24) rdw(std::integral_constant<int, kb + 2>{});
#pragma unroll
      for (int T = 0; T < 4; ++T) oc[T] = MFMA(wf[kb % 3][T], hf[kb], oc[T]);
#pragma unroll
      for (int T = 0; T < 4; ++T) { __builtin_amdgcn_sched_group_barrier(0x8, 1, 0); __builtin_amdgcn_sched_group_barrier(0x100, 2, 0); }
      SCHED();
    });
    TSTAMP(13);
#pragma unroll
    for (int h = 0; h < 2; ++h) {
      float o[8];
#pragma unroll
      for (int r = 0; r < 4; ++r) {
        unsigned a = __float_as_uint(oc[2 * h][r]), b = __float_as_uint(oc[2 * h + 1][r]);
        swap16(a, b);
        o[r] = __uint_as_float(a); o[4 + r] = __uint_as_float(b);
      }
      unsigned res[4];
#pragma unroll
      for (int d = 0; d < 4; ++d) {
        const float yl = (bflo(yv[0][h][d]) + bflo(yv[1][h][d])) - (bflo(yv[2][h][d]) + bflo(yv[3][h][d]));
        const float yh = (bfhi(yv[0][h][d]) + bfhi(yv[1][h][d])) - (bfhi(yv[2][h][d]) + bfhi(yv[3][h][d]));
        res[d] = pack2(bflo(xv[h][d]) + yl * silu(o[2 * d]), bfhi(xv[h][d]) + yh * silu(o[2 * d + 1]));
      }
      *(u32x4*)(out + eo + h * 64) = u32x4{res[0], res[1], res[2], res[3]};
    }
  }
}

__global__ __launch_bounds__(512, 2) void kF(const char* __restrict__ x, const char* __restrict__ wsre, const char* __restrict__ wsim,
                                             const char* __restrict__ wdre, const char* __restrict__ wdim, const char* __restrict__ win,
                                             const char* __restrict__ wsp, const char* __restrict__ bsp, const char* __restrict__ wout,
                                             char* __restrict__ ws, char* __restrict__ out) {
  extern __shared__ __attribute__((aligned(1024))) char shm[];
  asm volatile("" :: "s"(x), "s"(wsre), "s"(wsim), "s"(wdre), "s"(wdim), "s"(win), "s"(wsp), "s"(bsp), "s"(wout), "s"(ws), "s"(out));
  TSTAMP(0);
  const int xcd = blockIdx.x & 7;
  if (xcd < 4) body<true>(x, wdre, wsp, win, bsp, wout, ws, out, shm);
  else body<false>(x, wdim, (xcd & 2) ? wsim : wsre, win, bsp, wout, ws, out, shm);
  TSTAMP(1);
}

extern "C" void kernel_launch(void* const* d_in, const int* in_sizes, int n_in, void* d_out, int out_size, void* d_ws, size_t ws_size,
                              hipStream_t stream) {
  (void)hipFuncSetAttribute((const void*)kF, hipFuncAttributeMaxDynamicSharedMemorySize, LDS_BYTES);
  kF<<<256, 512, LDS_BYTES, stream>>>((const char*)d_in[0], (const char*)d_in[1], (const char*)d_in[2], (const char*)d_in[3],
                                      (const char*)d_in[4], (const char*)d_in[6], (const char*)d_in[7], (const char*)d_in[8],
                                      (const char*)d_in[9], (char*)d_ws, (char*)d_out);
}
```

```cpp
#include <hip/hip_runtime.h>
#include <hip/hip_bf16.h>
#include <stdint.h>
#include <type_traits>

typedef short bf16x8 __attribute__((ext_vector_type(8)));
typedef short bf16x4 __attribute__((ext_vector_type(4)));
typedef short v4i16_t __attribute__((ext_vector_type(4)));
typedef float f32x4 __attribute__((ext_vector_type(4)));
typedef float f32x2 __attribute__((ext_vector_type(2)));
typedef unsigned u32x4 __attribute__((ext_vector_type(4)));
typedef unsigned u32x2 __attribute__((ext_vector_type(2)));
typedef __bf16 bf16v2 __attribute__((ext_vector_type(2)));
typedef const __attribute__((address_space(1))) void* gptr_t;
typedef __attribute__((address_space(3))) void* lptr_t;
typedef __amdgpu_buffer_rsrc_t rsrc_t;

#define WAIT_V(n) asm volatile("s_waitcnt vmcnt(%0)" ::"n"(n) : "memory")
#define WAIT_L0() asm volatile("s_waitcnt lgkmcnt(0)" ::: "memory")
#define BAR() do { asm volatile("" ::: "memory"); __builtin_amdgcn_s_barrier(); asm volatile("" ::: "memory"); } while (0)
#define SCHED() __builtin_amdgcn_sched_barrier(0)
#define MFMA(a, b, c) __builtin_amdgcn_mfma_f32_16x16x32_bf16(a, b, c, 0, 0, 0)

#define PG_A 7
#define PG_T0 0
#define PG_DT 1
#define PG_N 7
#define PG_BS 1
#define PY_A 0
#define PY_T0 1
#define PY_DT 2
#define PY_I0 0
#define PY_N 0
#define PF_AUX 0
#define OUT_AUX 16
constexpr int S_ = 2048, D_ = 512, F_ = 1376, FH = 688, NFT = 43;
constexpr int NT_ALL = 112;
constexpr unsigned WS_BIMG = 0;
constexpr unsigned WS_Z1 = 8u << 20;
constexpr unsigned WS_H = 12u << 20;
constexpr unsigned WS_Y = 16u << 20;
constexpr unsigned WS_XST = 24u << 20;
constexpr unsigned WS_ZST = WS_XST + 65536;
constexpr unsigned WS_FLG = WS_ZST + 262144;
constexpr unsigned WS_SALT = WS_FLG + 131072;
constexpr unsigned WS_END = 25u << 20;
constexpr unsigned WS_DBG = 26u << 20;
constexpr int FL_AB = 0, FL_Z2 = 256, FL_Z1 = 384, FL_H = 512, FL_Y = 640;

template <int I, int N, class F>
__device__ __forceinline__ void static_for(F&& f) {
  if constexpr (I < N) { f(std::integral_constant<int, I>{}); static_for<I + 1, N>(f); }
}
__device__ __forceinline__ unsigned pack2(float a, float b) {
  return __builtin_bit_cast(unsigned, __builtin_convertvector(f32x2{a, b}, bf16v2));
}
__device__ __forceinline__ float bflo(unsigned u) { return __uint_as_float(u << 16); }
__device__ __forceinline__ float bfhi(unsigned u) { return __uint_as_float(u & 0xffff0000u); }
template <int AUX = 0>
__device__ __forceinline__ void glds16o(const char* ubase, unsigned off, char* l) {
  asm volatile("" : "+v"(off));
  __builtin_amdgcn_global_load_lds((gptr_t)(ubase + off), (lptr_t)l, 16, 0, AUX);
}
__device__ __forceinline__ void swap16(unsigned& a, unsigned& b) {
  auto r = __builtin_amdgcn_permlane16_swap(a, b, false, false);
  a = r[0]; b = r[1];
}
template <int CTRL>
__device__ __forceinline__ float dpp_add(float v) {
  return v + __int_as_float(__builtin_amdgcn_update_dpp(0, __float_as_int(v), CTRL, 0xf, 0xf, true));
}
__device__ __forceinline__ float row16_sum(float v) {
  v = dpp_add<0x128>(v); v = dpp_add<0x124>(v); v = dpp_add<0x122>(v); v = dpp_add<0x121>(v);
  return v;
}
__device__ __forceinline__ float wave_sum(float v) {
  v = row16_sum(v);
  const int i = __float_as_int(v);
  return __int_as_float(__builtin_amdgcn_readlane(i, 0)) + __int_as_float(__builtin_amdgcn_readlane(i, 16)) +
         __int_as_float(__builtin_amdgcn_readlane(i, 32)) + __int_as_float(__builtin_amdgcn_readlane(i, 48));
}
__device__ __forceinline__ float gelu_tanh(float v) {
  const float c0 = -2.3022081986f, c1 = -0.1029432397f;
  const float e = __builtin_amdgcn_exp2f(v * __builtin_fmaf(v * v, c1, c0));
  return v * __builtin_amdgcn_rcpf(1.0f + e);
}
__device__ __forceinline__ float silu(float v) {
  const float e = __builtin_amdgcn_exp2f(v * -1.4426950409f);
  return v * __builtin_amdgcn_rcpf(1.0f + e);
}
__device__ __forceinline__ bf16x8 lds_frag(const char* p) { return *(const bf16x8*)p; }
__device__ __forceinline__ bf16x4 lds_tr(const char* p) {
  return __builtin_bit_cast(bf16x4, __builtin_amdgcn_ds_read_tr16_b64_v4i16((__attribute__((address_space(3))) v4i16_t*)p));
}
__device__ __forceinline__ bf16x8 cat(bf16x4 a, bf16x4 b) { return __builtin_shufflevector(a, b, 0, 1, 2, 3, 4, 5, 6, 7); }

constexpr int SC1 = 16;
__device__ __forceinline__ void st16(rsrc_t rs, unsigned voff, unsigned soff, u32x4 v) { __builtin_amdgcn_raw_buffer_store_b128(v, rs, voff, soff, SC1); }
__device__ __forceinline__ u32x4 ld16(rsrc_t rs, unsigned voff, unsigned soff) { return __builtin_amdgcn_raw_buffer_load_b128(rs, voff, soff, SC1); }
__device__ __forceinline__ void granule_store(rsrc_t rs, unsigned off, float v0, float v1, u32x2 tag) {
  st16(rs, off, 0, u32x4{__float_as_uint(v0), tag[0], __float_as_uint(v1), tag[1]});
}
constexpr unsigned SPIN_MAX = 1u << 18;
__device__ __forceinline__ void flag_set(rsrc_t rs, int idx, u32x2 tag, int lane) {
  if (lane == 0) __builtin_amdgcn_raw_buffer_store_b64(tag, rs, WS_FLG + idx * 128, 0, SC1);
}
__device__ __forceinline__ void flag_wait(rsrc_t rs, int idx, u32x2 tag) {
  for (unsigned spin = 0; spin < SPIN_MAX; ++spin) {
    const u32x2 g = __builtin_amdgcn_raw_buffer_load_b64(rs, WS_FLG + idx * 128, 0, SC1);
    if (__all((g[0] == tag[0]) & (g[1] == tag[1]))) break;
    __builtin_amdgcn_s_sleep(1);
  }
  asm volatile("" ::: "memory");
}

#define TSTAMPC(slot) do {} while (0)
#define TSTAMP(slot) do {} while (0)
#define TSTAMPW(slot, wv) do {} while (0)

__device__ __forceinline__ constexpr int part_start(int p) { return p < 3 ? 6 * p : 18 + 5 * (p - 3); }
__device__ __forceinline__ constexpr int part_cnt(int p) { return p < 3 ? 6 : 5; }

constexpr int A_STAGE = 36864, A_NS = 4, A_XO = 0, A_DO = 16384, A_WO = 24576;
constexpr int L_HO = 69632;
constexpr int L_CS = A_NS * A_STAGE;
constexpr int L_XRS = L_CS + 2048;
constexpr int L_XNM = L_XRS + 512;
constexpr int L_ZPS = L_XNM + 512;
constexpr int L_ZPQ = L_ZPS + 1024;
constexpr int L_ZRS = L_ZPQ + 1024;
constexpr int L_ZNM = L_ZRS + 512;
constexpr int L_XROW = L_CS;
constexpr int L_DUM = L_XROW + 8192;
constexpr int L_SALT = L_DUM + 1024;
constexpr int LDS_BYTES = L_SALT + 1024;
static_assert(L_ZNM + 512 <= L_DUM, "");

template <bool G>
struct PB {
  static constexpr int NA = G ? 4 : 8, NB = G ? 3 : 4, NP = NA + NB;
  static constexpr int NS = G ? 5 : 3, NSTG = G ? 32 : 16, PF = G ? 4 : 2;
  static constexpr int ABYTES = NA * 4096, BBYTES = NB * 4096, BOFF = G ? 81920 : 98304;
  static constexpr int MT = G ? 2 : 8, NT = G ? 6 : 4, BKS = BBYTES / 2;
  static constexpr int cnt(int s) { return s >= NSTG ? 0 : (s < PF ? NB : NP); }
  static constexpr int span(int s0, int s1) { return s0 > s1 ? 0 : cnt(s0) + span(s0 + 1, s1); }
};

__device__ __forceinline__ void a_offsets(unsigned (&oA)[9], int pw, int lane, int panel, int j, bool z2) {
  const int part = j & 7, fstart = part_start(part), fcnt = part_cnt(part);
#pragma unroll
  for (int i = 0; i < 4; ++i) {
    const int row = 8 * (pw * 4 + i) + (lane >> 3);
    oA[i] = (unsigned)(panel * 128 + row) * 1024u + (unsigned)(((lane & 7) ^ ((row >> 1) & 7)) << 4);
  }
#pragma unroll
  for (int i = 0; i < 2; ++i) {
    const int row = 8 * (pw * 2 + i) + (lane >> 3);
    oA[4 + i] = (unsigned)((j & 7) * 64 + row) * 1024u + (unsigned)(((lane & 7) ^ ((row >> 1) & 7)) << 4);
  }
#pragma unroll
  for (int i = 0; i < 3; ++i) {
    const int o = (pw * 3 + i) * 1024 + lane * 16, row = o / 192, ch = (o % 192) >> 4;
    const int u = (ch >> 1) ^ ((row >> 3) & 1);
    const int ft = fstart + (u < fcnt ? u : fcnt - 1);
    oA[6 + i] = (unsigned)row * 2752u + (unsigned)((z2 ? FH * 2 : 0) + ft * 32 + (ch & 1) * 16);
  }
}

template <bool Z2>
__device__ __forceinline__ void body(const char* __restrict__ x, const char* __restrict__ wd, const char* __restrict__ wsq,
                                     const char* __restrict__ win, const char* __restrict__ bsp, const char* __restrict__ wout,
                                     char* __restrict__ ws, char* __restrict__ out, char* shm) {
  typedef PB<Z2> P;
  const int tid = threadIdx.x, w = __builtin_amdgcn_readfirstlane(tid >> 6), lane = tid & 63;
  const int c = lane & 15, q = lane >> 4;
  const int xcd = blockIdx.x & 7, bi = blockIdx.x >> 3;
  const bool prod = w >= 4;
  const int pw = w & 3, wr = pw >> 1, wc = pw & 1;
  const int panel = (xcd & 1) * 8 + (bi >> 2), j = (xcd >> 1) * 4 + (bi & 3);
  const int part = j & 7, fstart = part_start(part), fcnt = part_cnt(part);
  const int gpanel = xcd * 4 + (bi >> 3), gpart = bi & 7, gstart = part_start(gpart), gcnt = part_cnt(gpart);
  const int ymat = (xcd >> 1) & 1, ykh = xcd & 1, yp = bi >> 2, yeg = bi & 3;
  const int tb = xcd * 4 + (bi >> 3), G = bi & 7;

  const rsrc_t rs = __builtin_amdgcn_make_buffer_rsrc((void*)ws, 0, WS_END, 0x00020000);
  u32x2 tag = {0, 0};
  f32x4 ac[2][5];
  unsigned oa[P::NA], ob[P::NB];
  const char* Ab = wsq + (Z2 ? (size_t)gpanel * 128 * 4096 : (size_t)yp * 256 * 4096 + ykh * 2048);
  const char* Bb = ws + WS_BIMG + (Z2 ? 0 : (size_t)ykh * 32 * (NT_ALL * 1024));
  auto issueBA = [&](int t, int slot) {
    const char* ab = Ab + t * 128;
    asm volatile("" : "+s"(ab));
#pragma unroll
    for (int i = 0; i < P::NA; ++i) glds16o(ab, oa[i], shm + slot * P::ABYTES + (pw * P::NA + i) * 1024);
  };
  auto issueBB = [&](int t, int slot) {
    const char* bb = Bb + (size_t)t * (2 * NT_ALL * 1024);
    asm volatile("" : "+s"(bb));
#pragma unroll
    for (int i = 0; i < P::NB; ++i) glds16o<SC1>(bb, ob[i], shm + P::BOFF + slot * P::BBYTES + (pw * P::NB + i) * 1024);
  };

  auto touch = [&](const char* ubase, unsigned off) {
    asm volatile("" : "+v"(off));
    __builtin_amdgcn_global_load_lds((gptr_t)(ubase + off), (lptr_t)(shm + L_DUM + pw * 256), 4, 0, PF_AUX);
  };
  const unsigned tw0 = Z2 ? (unsigned)(gpanel * 128 + gpart * 16 + (lane >> 2)) * 4096u + (lane & 3) * 128
                          : (unsigned)(yp * 256 + yeg * 64 + (lane >> 2)) * 4096u + ykh * 2048 + (lane & 3) * 128;
  auto touchW = [&](int i) { touch(wsq, tw0 + (Z2 ? (unsigned)i * 512u : (unsigned)(i >> 2) * 512u + (unsigned)(i & 3) * 65536u)); };
  constexpr int TW_A = Z2 ? PG_A : PY_A, TW_T0 = Z2 ? PG_T0 : PY_T0, TW_DT = Z2 ? PG_DT : PY_DT, TW_I0 = Z2 ? 1 : PY_I0, TW_N = Z2 ? PG_N + 1 : PY_N;

  float* csum = (float*)(shm + L_CS);
  float* xrs = (float*)(shm + L_XRS);
  float* xnm = (float*)(shm + L_XNM);
  if (prod) {
    unsigned oA[9];
    a_offsets(oA, pw, lane, panel, j, Z2);
    auto issue = [&](int t) {
      char* st = shm + (t % A_NS) * A_STAGE;
      const char* xb = x + t * 128;
      const char* db = wd + t * 128;
      const char* wb = win + (size_t)t * (64 * 2752);
      asm volatile("" : "+s"(xb), "+s"(db), "+s"(wb));
#pragma unroll
      for (int i = 0; i < 4; ++i) glds16o(xb, oA[i], st + A_XO + (pw * 4 + i) * 1024);
#pragma unroll
      for (int i = 0; i < 2; ++i) glds16o(db, oA[4 + i], st + A_DO + (pw * 2 + i) * 1024);
#pragma unroll
      for (int i = 0; i < 3; ++i) glds16o(wb, oA[6 + i], st + A_WO + (pw * 3 + i) * 1024);
    };
#pragma unroll
    for (int t = 1; t < A_NS; ++t) issue(t);
#pragma unroll
    for (int i = 0; i < P::NA; ++i) {
      const int row = (pw * P::NA + i) * 8 + (lane >> 3);
      oa[i] = (unsigned)row * 4096u + (unsigned)(((lane & 7) ^ ((row >> 1) & 7)) << 4);
    }
#pragma unroll
    for (int i = 0; i < P::NB; ++i) {
      const int n = (pw & 1) * P::NB + i;
      const int nt = Z2 ? 64 + gstart + (n < gcnt ? n : gcnt - 1) : ymat * 32 + yeg * 8 + n;
      ob[i] = (unsigned)(((pw >> 1) * NT_ALL + nt) * 1024 + lane * 16);
    }
    WAIT_V(27);
    BAR();
    {
      const u32x2 salt = *(const u32x2*)(shm + L_SALT + pw * 256);
      const unsigned long long tag64 = (((unsigned long long)salt[1] << 32) | salt[0]) + 0x9E3779B97F4A7C15ull;
      tag = u32x2{(unsigned)tag64, (unsigned)(tag64 >> 32)};
    }
    const unsigned gxo = WS_XST + (panel * 128 + pw * 32 + (lane & 31)) * 16;
    u32x4 gx;
    static_for<0, 7>([&](auto T) {
      constexpr int t = T.value;
      if constexpr (t < 6) WAIT_V(t <= 4 ? 18 : 9);
      else asm volatile("s_waitcnt vmcnt(%1)" : "+v"(gx) : "n"(P::PF * P::NA) : "memory");
      BAR();
      if constexpr (t + A_NS < 8) issue(t + A_NS);
      if constexpr (t == 5) {
        const char* pg = ws + gxo;
        asm volatile("global_load_dwordx4 %0, %1, off sc1" : "=&v"(gx) : "v"(pg) : "memory");
#pragma unroll
        for (int s = 0; s < P::PF; ++s) issueBA(s, s);
      }
    });
    if (!__all((gx[1] == tag[0]) & (gx[3] == tag[1]))) {
      for (unsigned spin = 0; spin < SPIN_MAX; ++spin) {
        gx = ld16(rs, gxo, 0);
        if (__all((gx[1] == tag[0]) & (gx[3] == tag[1]))) break;
        __builtin_amdgcn_s_sleep(1);
      }
    }
    xrs[pw * 32 + (lane & 31)] = __uint_as_float(gx[0]);
    xnm[pw * 32 + (lane & 31)] = __uint_as_float(gx[2]);
    WAIT_L0();
  } else {
    f32x4 acc[4][5] = {};
    f32x4 cs[5] = {};
    bf16x8 fx[2][4], fd[2][2], fw[2][3];
    const bf16x8 ones = {0x3F80, 0x3F80, 0x3F80, 0x3F80, 0x3F80, 0x3F80, 0x3F80, 0x3F80};
    int ax[2], ad[2], aw[3];
#pragma unroll
    for (int ks = 0; ks < 2; ++ks) {
      const int sw = ((ks * 4 + q) ^ ((c >> 1) & 7)) << 4;
      ax[ks] = A_XO + (64 * wr + c) * 128 + sw;
      ad[ks] = A_DO + (32 * wc + c) * 128 + sw;
    }
#pragma unroll
    for (int i = 0; i < 3; ++i) aw[i] = A_WO + (8 * q + (c >> 2)) * 192 + (((3 * wc + i) ^ (q & 1)) << 5) + (c & 3) * 8;
    if (w == 0) WAIT_V(0);
    else WAIT_V(1);
    {
      const u32x2 salt = *(const u32x2*)(shm + L_SALT + pw * 256);
      const u32x4 xr0 = *(const u32x4*)(shm + L_XROW + pw * 2048 + lane * 16), xr1 = *(const u32x4*)(shm + L_XROW + pw * 2048 + 1024 + lane * 16);
      const unsigned long long tag64 = (((unsigned long long)salt[1] << 32) | salt[0]) + 0x9E3779B97F4A7C15ull;
      tag = u32x2{(unsigned)tag64, (unsigned)(tag64 >> 32)};
      float v[2][8], mean[2], ss[2];
#pragma unroll
      for (int i = 0; i < 4; ++i) { v[0][2 * i] = bflo(xr0[i]); v[0][2 * i + 1] = bfhi(xr0[i]); v[1][2 * i] = bflo(xr1[i]); v[1][2 * i + 1] = bfhi(xr1[i]); }
#pragma unroll
      for (int r = 0; r < 2; ++r)
        mean[r] = wave_sum(((v[r][0] + v[r][1]) + (v[r][2] + v[r][3])) + ((v[r][4] + v[r][5]) + (v[r][6] + v[r][7]))) * (1.0f / D_);
#pragma unroll
      for (int r = 0; r < 2; ++r) {
        ss[r] = 0.f;
#pragma unroll
        for (int i = 0; i < 8; ++i) { const float d = v[r][i] - mean[r]; ss[r] = __builtin_fmaf(d, d, ss[r]); }
        ss[r] = __builtin_amdgcn_rsqf(wave_sum(ss[r]) * (1.0f / D_) + 1e-5f);
      }
      const int r = lane & 1;
      const float rsv = r ? ss[1] : ss[0], mv = r ? mean[1] : mean[0];
      if (lane < 2) granule_store(rs, WS_XST + (panel * 128 + 8 * j + 2 * w + r) * 16, rsv, -mv * rsv, tag);
    }
    auto rd = [&](int set, int t, int ks) {
      int so = (t % A_NS) * A_STAGE;
      if (so != 0) asm volatile("" : "+s"(so));
      const char* st = shm + so;
#pragma unroll
      for (int i = 0; i < 2; ++i) fd[set][i] = lds_frag(st + ad[ks] + i * 2048);
#pragma unroll
      for (int m = 0; m < 4; ++m) fx[set][m] = lds_frag(st + ax[ks] + m * 2048);
#pragma unroll
      for (int i = 0; i < 3; ++i) fw[set][i] = cat(lds_tr(st + aw[i] + ks * 6144), lds_tr(st + aw[i] + ks * 6144 + 768));
    };
    auto mm = [&](int set) {
#pragma unroll
      for (int i = 0; i < 2; ++i)
#pragma unroll
        for (int m = 0; m < 4; ++m) acc[m][i] = MFMA(fx[set][m], fd[set][i], acc[m][i]);
#pragma unroll
      for (int i = 0; i < 3; ++i)
#pragma unroll
        for (int m = 0; m < 4; ++m)
          acc[m][2 + i] = Z2 ? MFMA(fx[set][m], fw[set][i], acc[m][2 + i]) : MFMA(fw[set][i], fx[set][m], acc[m][2 + i]);
    };
    auto mmcs = [&](int set) {
#pragma unroll
      for (int i = 0; i < 2; ++i) cs[i] = MFMA(ones, fd[set][i], cs[i]);
#pragma unroll
      for (int i = 0; i < 3; ++i) cs[2 + i] = Z2 ? MFMA(ones, fw[set][i], cs[2 + i]) : MFMA(fw[set][i], ones, cs[2 + i]);
    };
    auto interleave = [&]() {
#pragma unroll
      for (int i = 0; i < 12; ++i) { __builtin_amdgcn_sched_group_barrier(0x8, 1, 0); __builtin_amdgcn_sched_group_barrier(0x100, 1, 0); }
      __builtin_amdgcn_sched_group_barrier(0x8, 8, 0);
    };
    BAR();
    TSTAMP(2);
    rd(0, 0, 0);
    WAIT_L0(); SCHED();
    static_for<0, 8>([&](auto T) {
      constexpr int t = T.value;
      if constexpr (t >= TW_T0 && (t - TW_T0) % TW_DT == 0 && (t - TW_T0) / TW_DT < TW_A) { if (w == 1) touchW(TW_I0 + (t - TW_T0) / TW_DT); }
      rd(1, t, 1); mm(0); interleave(); SCHED();
      if (wr == 0) mmcs(0);
      if constexpr (t < 7) {
        WAIT_L0(); BAR(); SCHED();
        rd(0, t + 1, 0); mm(1); interleave(); SCHED();
      } else {
        mm(1);
      }
      if (wr == 1) mmcs(1);
    });
    TSTAMP(3);
#pragma unroll
    for (int m = 0; m < 2; ++m)
#pragma unroll
      for (int i = 0; i < 5; ++i) {
        *(f32x4*)(shm + L_HO + (pw * 10 + m * 5 + i) * 1024 + lane * 16) = acc[2 + m][i];
        ac[m][i] = acc[m][i];
      }
#pragma unroll
    for (int i = 0; i < 5; ++i) {
      if (i < 2 || Z2) { if (q == 0) csum[((wc * 5 + i) * 16 + c) * 2 + wr] = cs[i][0]; }
      else if (c == 0) {
#pragma unroll
        for (int r = 0; r < 4; ++r) csum[((wc * 5 + i) * 16 + 4 * q + r) * 2 + wr] = cs[i][r];
      }
    }
    WAIT_L0();
  }
  BAR();
  TSTAMP(4);

  {
    const int wr4 = 2 * wr + (w >> 2);
    if (prod) {
#pragma unroll
      for (int m = 0; m < 2; ++m)
#pragma unroll
        for (int i = 0; i < 5; ++i) ac[m][i] = *(const f32x4*)(shm + L_HO + (pw * 10 + m * 5 + i) * 1024 + lane * 16);
    }
    const unsigned bimg = WS_BIMG + (unsigned)(panel * 4 + wr4) * (NT_ALL * 1024) + (c + 16 * (((q & 1) << 1) | (q >> 1))) * 16;
    f32x4 rs4[2], nm4[2];
#pragma unroll
    for (int m = 0; m < 2; ++m) {
      rs4[m] = *(const f32x4*)(xrs + 32 * wr4 + 16 * m + 4 * q);
      nm4[m] = *(const f32x4*)(xnm + 32 * wr4 + 16 * m + 4 * q);
    }
    auto colsum1 = [&](int i) { const f32x2 t = *(const f32x2*)(csum + ((wc * 5 + i) * 16 + c) * 2); return t[0] + t[1]; };
    auto store_bt = [&](const f32x4& v0, const f32x4& v1, int ntile) {
      unsigned a0 = pack2(v0[0], v0[1]), a1 = pack2(v0[2], v0[3]), b0 = pack2(v1[0], v1[1]), b1 = pack2(v1[2], v1[3]);
      swap16(a0, b0); swap16(a1, b1);
      st16(rs, bimg + ntile * 1024, 0, u32x4{a0, a1, b0, b1});
    };
#pragma unroll
    for (int i = 0; i < 2; ++i) {
      const float cv = colsum1(i);
      store_bt(rs4[0] * ac[0][i] + nm4[0] * cv, rs4[1] * ac[1][i] + nm4[1] * cv, (j >> 3) * 32 + (j & 7) * 4 + 2 * wc + i);
    }

    if constexpr (!Z2) {
      float rl[2], nl[2];
#pragma unroll
      for (int m = 0; m < 2; ++m) { rl[m] = xrs[32 * wr4 + 16 * m + c]; nl[m] = xnm[32 * wr4 + 16 * m + c]; }
      u32x4 zp[3];
#pragma unroll
      for (int i = 0; i < 3; ++i) {
        f32x4 cv;
#pragma unroll
        for (int r = 0; r < 4; ++r) { const f32x2 t = *(const f32x2*)(csum + ((wc * 5 + 2 + i) * 16 + 4 * q + r) * 2); cv[r] = t[0] + t[1]; }
#pragma unroll
        for (int m = 0; m < 2; ++m) {
          const f32x4 v = rl[m] * ac[m][2 + i] + nl[m] * cv;
          zp[i][2 * m] = pack2(gelu_tanh(v[0]), gelu_tanh(v[1]));
          zp[i][2 * m + 1] = pack2(gelu_tanh(v[2]), gelu_tanh(v[3]));
        }
      }
      asm volatile("" : "+v"(zp[0]), "+v"(zp[1]), "+v"(zp[2]));
      WAIT_V(0); BAR();
      TSTAMP(5);
      if (w == 0) flag_set(rs, FL_AB + panel * 16 + j, tag, lane);
      const unsigned z1o = WS_Z1 + (unsigned)(panel * 4 + wr4) * (NFT * 1024) + lane * 16;
#pragma unroll
      for (int i = 0; i < 3; ++i)
        if (3 * wc + i < fcnt) st16(rs, z1o + (fstart + 3 * wc + i) * 1024, 0, zp[i]);
      if (w == 0) flag_wait(rs, FL_AB + (ykh * 8 + ((lane >> 1) & 7)) * 16 + ymat * 8 + 2 * yeg + (lane & 1), tag);
      WAIT_V(0); BAR();
      TSTAMP(8);
      if (w == 1) flag_set(rs, FL_Z1 + panel * 8 + part, tag, lane);
    } else {
      f32x4 zv[2][3];
      f32x4 ssum[2] = {}, ssq[2] = {};
#pragma unroll
      for (int i = 0; i < 3; ++i) {
        const float cv = colsum1(2 + i);
        const float valid = (3 * wc + i < fcnt) ? 1.0f : 0.0f;
#pragma unroll
        for (int m = 0; m < 2; ++m) {
          const f32x4 v = rs4[m] * ac[m][2 + i] + nm4[m] * cv;
#pragma unroll
          for (int r = 0; r < 4; ++r) zv[m][i][r] = gelu_tanh(v[r]) * valid;
          ssum[m] += zv[m][i];
          ssq[m] += zv[m][i] * zv[m][i];
        }
      }
      float* zps = (float*)(shm + L_ZPS);
      float* zpq = (float*)(shm + L_ZPQ);
      float* zrs = (float*)(shm + L_ZRS);
      float* znm = (float*)(shm + L_ZNM);
#pragma unroll
      for (int m = 0; m < 2; ++m) {
#pragma unroll
        for (int r = 0; r < 4; ++r) { ssum[m][r] = row16_sum(ssum[m][r]); ssq[m][r] = row16_sum(ssq[m][r]); }
        if (c == 0) {
          *(f32x4*)(zps + wc * 128 + 32 * wr4 + 16 * m + 4 * q) = ssum[m];
          *(f32x4*)(zpq + wc * 128 + 32 * wr4 + 16 * m + 4 * q) = ssq[m];
        }
      }
      WAIT_V(0); WAIT_L0(); BAR();
      TSTAMP(5);
      if (w == 2) flag_set(rs, FL_AB + panel * 16 + j, tag, lane);
      if (w < 2) {
        const unsigned zoff = WS_ZST + (panel * 8 * 128 + tid) * 16;
        granule_store(rs, zoff + part * 2048, zps[tid] + zps[128 + tid], zpq[tid] + zpq[128 + tid], tag);
        for (unsigned spin = 0; spin < SPIN_MAX; ++spin) {
          bool ok = true; float s = 0.f, sq = 0.f;
#pragma unroll
          for (int p = 0; p < 8; ++p) {
            const u32x4 g = ld16(rs, zoff + p * 2048, 0);
            ok = ok & (g[1] == tag[0]) & (g[3] == tag[1]);
            s += __uint_as_float(g[0]); sq += __uint_as_float(g[2]);
          }
          const float mean = s * (1.0f / FH);
          const float var = __builtin_fmaxf(sq * (1.0f / FH) - mean * mean, 0.0f);
          const float rsv = __builtin_amdgcn_rsqf(var + 1e-5f);
          zrs[tid] = rsv; znm[tid] = -mean * rsv;
          if (__all(ok)) break;
          __builtin_amdgcn_s_sleep(1);
        }
      }
      WAIT_L0(); BAR();
      TSTAMP(6);
      f32x4 zr[2], zn[2];
#pragma unroll
      for (int m = 0; m < 2; ++m) {
        zr[m] = *(const f32x4*)(zrs + 32 * wr4 + 16 * m + 4 * q);
        zn[m] = *(const f32x4*)(znm + 32 * wr4 + 16 * m + 4 * q);
      }
#pragma unroll
      for (int i = 0; i < 3; ++i)
        if (3 * wc + i < fcnt) store_bt(zv[0][i] * zr[0] + zn[0], zv[1][i] * zr[1] + zn[1], 64 + fstart + 3 * wc + i);
      WAIT_V(0); BAR();
      TSTAMP(7);
      if (w == 0) {
        flag_set(rs, FL_Z2 + panel * 8 + part, tag, lane);
        flag_wait(rs, lane < 16 ? FL_Z2 + lane * 8 + gpart : FL_Z1 + gpanel * 8 + gpart, tag);
      }
      BAR();
      TSTAMP(8);
    }
  }

  if (prod) {
#pragma unroll
    for (int s = 0; s < P::PF; ++s) issueBB(s, s);
#pragma unroll
    for (int s = P::PF; s < P::NS; ++s) { issueBA(s, s); issueBB(s, s); }
    WAIT_V(P::span(1, P::NS - 1)); BAR();
    constexpr int NPEEL = P::PF > 2 ? P::PF - 2 : 0;
    static_for<0, NPEEL>([&](auto T) {
      constexpr int t = T.value;
      WAIT_V(P::span(t + 2, t + P::NS - 1)); BAR();
      issueBA(t + P::NS, t % P::NS); issueBB(t + P::NS, t % P::NS);
    });
    int slot = NPEEL % P::NS;
    for (int t = NPEEL; t < P::NSTG - P::NS; ++t) {
      WAIT_V((P::NS - 2) * P::NP); BAR();
      issueBA(t + P::NS, slot); issueBB(t + P::NS, slot);
      slot = (slot + 1 == P::NS) ? 0 : slot + 1;
    }
    static_for<0, P::NS>([&](auto I) {
      constexpr int rem = P::NS - 2 - I.value;
      WAIT_V((rem > 0 ? rem : 0) * P::NP); BAR();
    });
    const int rr = lane >> 3;
    const unsigned lo = (unsigned)rr * 1024u + (unsigned)(G * 128 + (((lane & 7) ^ (2 * ((rr >> 1) & 3))) << 4));
#pragma unroll
    for (int k = 0; k < 22; ++k) {
      if (k == 10) BAR();
      int Pc = 4 * k + pw; Pc = Pc < 85 ? Pc : 85;
      glds16o(wout + (size_t)Pc * 8192, lo, shm + Pc * 1024);
    }
    WAIT_V(0); BAR();
    if constexpr (!Z2) asm volatile(".Lkf_end1:");
  } else {
    f32x4 acc[P::MT][P::NT] = {};
    u32x4 z1[6]; float bs[2];
    if constexpr (Z2) {
#pragma unroll
      for (int n = 0; n < 6; ++n)
        z1[n] = ld16(rs, WS_Z1 + (unsigned)((gpanel * 4 + w) * NFT + gstart + (n < gcnt ? n : gcnt - 1)) * 1024 + lane * 16, 0);
#pragma unroll
      for (int m = 0; m < 2; ++m) bs[m] = bflo(*(const unsigned short*)(bsp + (gpanel * 128 + w * 32 + m * 16 + c) * 2));
    }
    {
      constexpr int MT = P::MT, NT = P::NT;
      bf16x8 fa[2][MT], fb[2][NT];
      int aoff[2];
#pragma unroll
      for (int ks = 0; ks < 2; ++ks) aoff[ks] = ((Z2 ? 32 * w : 128 * wr) + c) * 128 + (((ks * 4 + q) ^ ((c >> 1) & 7)) << 4);
      const int boff = P::BOFF + (Z2 ? 0 : wc * 4096) + lane * 16;
      auto rd = [&](int set, int slot, int ks) {
        const char* sa = shm + slot * P::ABYTES;
        const char* sb = shm + slot * P::BBYTES;
#pragma unroll
        for (int i = 0; i < (MT > NT ? MT : NT); ++i) {
          if (i < NT) fb[set][i] = lds_frag(sb + boff + ks * P::BKS + i * 1024);
          if (i < MT) fa[set][i] = lds_frag(sa + aoff[ks] + i * 2048);
        }
      };
      auto mm = [&](int set) {
#pragma unroll
        for (int m = 0; m < MT; ++m)
#pragma unroll
          for (int n = 0; n < NT; ++n) acc[m][n] = MFMA(fb[set][n], fa[set][m], acc[m][n]);
      };
      auto interleave = [&]() {
#pragma unroll
        for (int i = 0; i < MT + NT; ++i) { __builtin_amdgcn_sched_group_barrier(0x8, 1, 0); __builtin_amdgcn_sched_group_barrier(0x100, 1, 0); }
        __builtin_amdgcn_sched_group_barrier(0x8, MT * NT - MT - NT, 0);
      };
      BAR();
      TSTAMP(9); TSTAMPC(14);
      rd(0, 0, 0);
      WAIT_L0(); SCHED();
      int slot = 0;
      for (int t = 0; t < P::NSTG; ++t) {
        if constexpr (Z2) { if (w == 1 && (t & ((1 << PG_BS) - 1)) == 0 && TW_I0 + TW_A + (t >> PG_BS) < TW_N) touchW(TW_I0 + TW_A + (t >> PG_BS)); }
        else { if (w == 1 && TW_I0 + TW_A + t < TW_N) touchW(TW_I0 + TW_A + t); }
        rd(1, slot, 1); mm(0); interleave(); SCHED();
        WAIT_L0(); BAR(); SCHED();
        slot = (slot + 1 == P::NS) ? 0 : slot + 1;
        rd(0, slot, 0);
        mm(1); interleave(); SCHED();
      }
    }
    TSTAMP(10); TSTAMPC(15);
    if constexpr (Z2) {
#pragma unroll
      for (int m = 0; m < 2; ++m)
#pragma unroll
        for (int p = 0; p < 3; ++p) {
          u32x4 o;
#pragma unroll
          for (int h = 0; h < 2; ++h) {
            const int n = 2 * p + h;
            const unsigned zl = z1[n][2 * m], zh = z1[n][2 * m + 1];
            const f32x4 g = acc[m][n] + bs[m];
            o[2 * h] = pack2(bflo(zl) * g[0], bfhi(zl) * g[1]);
            o[2 * h + 1] = pack2(bflo(zh) * g[2], bfhi(zh) * g[3]);
          }
          if (p == 2 && gcnt < 6) { o[2] = 0; o[3] = 0; }
          st16(rs, WS_H + (unsigned)((gpanel * 8 + 2 * w + m) * 24 + gpart * 3 + p) * 1024 + lane * 16, 0, o);
        }
    } else {
      const unsigned yo = WS_Y + (unsigned)(((ymat * 2 + ykh) * 128 + yp * 16 + wr * 8) * 16 + yeg * 4 + wc * 2) * 1024 + lane * 16;
#pragma unroll
      for (int m = 0; m < 8; ++m)
#pragma unroll
        for (int h = 0; h < 2; ++h) {
          const f32x4 v0 = acc[m][2 * h], v1 = acc[m][2 * h + 1];
          unsigned a0 = pack2(v0[0], v0[1]), a1 = pack2(v0[2], v0[3]), b0 = pack2(v1[0], v1[1]), b1 = pack2(v1[2], v1[3]);
          swap16(a0, b0); swap16(a1, b1);
          st16(rs, yo + m * 16384 + h * 1024, 0, u32x4{a0, a1, b0, b1});
        }
    }
    WAIT_V(0); BAR();
    TSTAMP(11);
    if (w == 0) flag_set(rs, Z2 ? FL_H + gpanel * 8 + gpart : FL_Y + ((ymat * 2 + ykh) * 8 + yp) * 4 + yeg, tag, lane);
    const size_t eo = (size_t)(tb * 64 + w * 16 + c) * 1024 + (G * 64 + 16 * (q & 1) + 8 * (q >> 1)) * 2;
    u32x4 xv[2];
#pragma unroll
    for (int h = 0; h < 2; ++h) xv[h] = *(const u32x4*)(x + eo + h * 64);
    if (w == 0) flag_wait(rs, lane < 8 ? FL_H + (tb >> 1) * 8 + lane : FL_Y + (((lane - 8) & 3) * 8 + (tb >> 2)) * 4 + (G >> 1), tag);
    if (w == 1 && blockIdx.x == 0) {
#pragma unroll
      for (int k = 0; k < 4; ++k) flag_wait(rs, FL_AB + k * 64 + lane, tag);
      if (lane == 0) __builtin_amdgcn_raw_buffer_store_b64(tag, rs, WS_SALT, 0, SC1);
    }
    BAR();
    TSTAMP(12);
    bf16x8 hf[24];
    u32x4 yv[4][2];
#pragma unroll
    for (int kb = 0; kb < 24; ++kb) hf[kb] = __builtin_bit_cast(bf16x8, ld16(rs, WS_H + (unsigned)((tb * 4 + w) * 24 + kb) * 1024 + lane * 16, 0));
#pragma unroll
    for (int h = 0; h < 2; ++h)
#pragma unroll
      for (int p = 0; p < 4; ++p) yv[p][h] = ld16(rs, WS_Y + (unsigned)((p * 128 + tb * 4 + w) * 16 + 2 * G + h) * 1024 + lane * 16, 0);
    int at[4];
    {
      const int r = c >> 2, sg = (2 * (q & 1) + (r >> 1)) & 3;
#pragma unroll
      for (int T = 0; T < 4; ++T) at[T] = (4 * q + r) * 128 + ((T ^ sg) << 5) + (c & 3) * 8;
    }
    f32x4 oc[4] = {};
    bf16x8 wf[3][4];
    auto rdw = [&](auto KB) {
      constexpr int kb = KB.value, pt = kb / 3, pr = kb % 3, ro = (part_start(pt) + 2 * pr) * 2048;
      constexpr bool full = 2 * pr + 1 < part_cnt(pt);
#pragma unroll
      for (int T = 0; T < 4; ++T) {
        const bf16x4 lo = lds_tr(shm + at[T] + ro);
        const bf16x4 hi = full ? lds_tr(shm + at[T] + ro + 2048) : bf16x4{0, 0, 0, 0};
        wf[kb % 3][T] = cat(lo, hi);
      }
    };
    rdw(std::integral_constant<int, 0>{});
    rdw(std::integral_constant<int, 1>{});
    SCHED();
    static_for<0, 24>([&](auto KB) {
      constexpr int kb = KB.value;
      if constexpr (kb + 2 < 24) rdw(std::integral_constant<int, kb + 2>{});
#pragma unroll
      for (int T = 0; T < 4; ++T) oc[T] = MFMA(wf[kb % 3][T], hf[kb], oc[T]);
#pragma unroll
      for (int T = 0; T < 4; ++T) { __builtin_amdgcn_sched_group_barrier(0x8, 1, 0); __builtin_amdgcn_sched_group_barrier(0x100, 2, 0); }
      SCHED();
    });
    TSTAMP(13);
#pragma unroll
    for (int h = 0; h < 2; ++h) {
      float o[8];
#pragma unroll
      for (int r = 0; r < 4; ++r) {
        unsigned a = __float_as_uint(oc[2 * h][r]), b = __float_as_uint(oc[2 * h + 1][r]);
        swap16(a, b);
        o[r] = __uint_as_float(a); o[4 + r] = __uint_as_float(b);
      }
      unsigned res[4];
#pragma unroll
      for (int d = 0; d < 4; ++d) {
        const float yl = (bflo(yv[0][h][d]) + bflo(yv[1][h][d])) - (bflo(yv[2][h][d]) + bflo(yv[3][h][d]));
        const float yh = (bfhi(yv[0][h][d]) + bfhi(yv[1][h][d])) - (bfhi(yv[2][h][d]) + bfhi(yv[3][h][d]));
        res[d] = pack2(bflo(xv[h][d]) + yl * silu(o[2 * d]), bfhi(xv[h][d]) + yh * silu(o[2 * d + 1]));
      }
      __builtin_amdgcn_raw_buffer_store_b128(u32x4{res[0], res[1], res[2], res[3]}, __builtin_amdgcn_make_buffer_rsrc((void*)out, 0, S_ * D_ * 2, 0x00020000), (unsigned)eo + h * 64, 0, OUT_AUX);
    }
    if constexpr (!Z2) asm volatile(".Lkf_end0:");

  }
}

__global__ __launch_bounds__(512, 2) void kF(const char* __restrict__ x, const char* __restrict__ wsre, const char* __restrict__ wsim,
                                             const char* __restrict__ wdre, const char* __restrict__ wdim, const char* __restrict__ win,
                                             const char* __restrict__ wsp, const char* __restrict__ bsp, const char* __restrict__ wout,
                                             char* __restrict__ ws, char* __restrict__ out) {
  extern __shared__ __attribute__((aligned(1024))) char shm[];
  TSTAMP(0);
  const char* code; unsigned code_len, len1;
  asm volatile("s_getpc_b64 %0\n.Lkf_base:\n\ts_mov_b32 %1, .Lkf_end0-.Lkf_base\n\ts_mov_b32 %2, .Lkf_end1-.Lkf_base" : "=s"(code), "=s"(code_len), "=s"(len1));
  const int xcd = blockIdx.x & 7, bi = blockIdx.x >> 3;
  const bool z2 = xcd < 4;
#define PIN_ARGS() asm volatile("" :: "s"(x), "s"(wsre), "s"(wsim), "s"(wdre), "s"(wdim), "s"(win), "s"(wsp), "s"(bsp), "s"(wout), "s"(ws), "s"(out))
  {
    const int w = __builtin_amdgcn_readfirstlane(threadIdx.x >> 6), pw = w & 3;
    int lane = threadIdx.x & 63;
    asm volatile("" : "+v"(lane));
    const int panel = (xcd & 1) * 8 + (bi >> 2), j = (xcd >> 1) * 4 + (bi & 3);
    int zsel = xcd >> 2;
    if (w >= 4) {
      unsigned oA[9];
      a_offsets(oA, pw, lane, panel, j, z2);
      asm volatile("" : "+v"(oA[0]), "+v"(oA[1]), "+v"(oA[2]), "+v"(oA[3]), "+v"(oA[4]), "+v"(oA[5]), "+v"(oA[6]), "+v"(oA[7]), "+v"(oA[8]), "+s"(zsel));
      PIN_ARGS();
      const char* wd = zsel ? wdim : wdre;
#pragma unroll
      for (int i = 0; i < 4; ++i) glds16o(x, oA[i], shm + A_XO + (pw * 4 + i) * 1024);
#pragma unroll
      for (int i = 0; i < 2; ++i) glds16o(wd, oA[4 + i], shm + A_DO + (pw * 2 + i) * 1024);
#pragma unroll
      for (int i = 0; i < 3; ++i) glds16o(win, oA[6 + i], shm + A_WO + (pw * 3 + i) * 1024);
    } else {
      unsigned so = WS_SALT + (lane & 1) * 4;
      unsigned xo = (unsigned)(panel * 128 + 8 * j + 2 * w) * 1024u + lane * 16;
      code_len = (int)code_len < (int)len1 ? len1 : code_len;
      code_len = (int)code_len < 64 ? 64 : (code_len & ~3u);
      const unsigned co = (unsigned)bi * 4096u + lane * 64;
      unsigned to = w == 1 ? (unsigned)((bi * 4 + (xcd & 3)) * 4) * 1024u + lane * 64
                  : w == 2 ? (unsigned)(256 + blockIdx.x) * 2752u + (lane < 43 ? lane : 42) * 64
                           : (co < code_len - 4 ? co : code_len - 4);
      asm volatile("" : "+v"(so), "+v"(xo), "+v"(to), "+s"(zsel));
      PIN_ARGS();
      __builtin_amdgcn_global_load_lds((gptr_t)(ws + so), (lptr_t)(shm + L_SALT + pw * 256), 4, 0, SC1);
      glds16o(x, xo, shm + L_XROW + pw * 2048);
      glds16o(x, xo + 1024, shm + L_XROW + pw * 2048 + 1024);
      if (w != 0) {
        const char* tb = w == 1 ? (zsel ? wdim : wdre) : w == 2 ? win : code;
        __builtin_amdgcn_global_load_lds((gptr_t)(tb + to), (lptr_t)(shm + L_DUM + pw * 256), 4, 0, 0);
      }
    }
  }
  const char* wd = z2 ? wdre : wdim;
  if (__builtin_expect(z2, 1)) body<true>(x, wd, wsp, win, bsp, wout, ws, out, shm);
  else body<false>(x, wd, (xcd & 2) ? wsim : wsre, win, bsp, wout, ws, out, shm);
  TSTAMP(1);
}

extern "C" void kernel_launch(void* const* d_in, const int* in_sizes, int n_in, void* d_out, int out_size, void* d_ws, size_t ws_size,
                              hipStream_t stream) {
  (void)hipFuncSetAttribute((const void*)kF, hipFuncAttributeMaxDynamicSharedMemorySize, LDS_BYTES);
  kF<<<256, 512, LDS_BYTES, stream>>>((const char*)d_in[0], (const char*)d_in[1], (const char*)d_in[2], (const char*)d_in[3],
                                      (const char*)d_in[4], (const char*)d_in[6], (const char*)d_in[7], (const char*)d_in[8],
                                      (const char*)d_in[9], (char*)d_ws, (char*)d_out);
}
```
